```python
import torch

def _softmax(x, axis=-1):
    return torch.softmax(x, dim=axis)
def _gelu(x):
    return torch.nn.functional.gelu(x, approximate="tanh")
def _silu(x):
    return torch.nn.functional.silu(x)
def _sigmoid(x):
    return torch.sigmoid(x)
def _layernorm(x, axis=-1, eps=1e-5):
    m = x.mean(dim=axis, keepdim=True)
    v = x.var(dim=axis, keepdim=True, unbiased=False)
    return (x - m) / torch.sqrt(v + eps)
def _scatter_add(tgt, idx, src, axis=0):
    return tgt.clone().index_add_(axis, idx, src)
def _segment_sum(src, seg, n):
    out = torch.zeros((n,) + src.shape[1:], dtype=src.dtype)
    return out.index_add_(0, seg, src)
def _pad(x, pw):
    flat = tuple(v for pair in reversed(pw) for v in pair)
    return torch.nn.functional.pad(x, flat)

kernel_name = 'recurrent_scan__sliding_block_cached__glu_gated_glu__prompt'

def setup_inputs():

    _w = (lambda a: (lambda t: t.to(torch.bfloat16) if t.is_floating_point() else t)(torch.as_tensor(a)))
    def _A_setup():
        B, Sq, D = 1, 2048, 640
        Hq, Hkv, Sk, Dh = 32, 32, 2048, 128
        assert Hq % Hkv == 0, (Hq, Hkv)
        return {
            "x": _w((lambda *s: torch.randn(*s, dtype=torch.bfloat16) * 0.05)(B, Sq, D)),
            "k_cache": _w((lambda *s: torch.randn(*s, dtype=torch.bfloat16) * 0.8)(B, Hkv, Sk, Dh)),
            "v_cache": _w((lambda *s: torch.randn(*s, dtype=torch.bfloat16) * 0.2)(B, Hkv, Sk, Dh)),
            "wq": _w((lambda *s: torch.randn(*s, dtype=torch.bfloat16) * 0.8)(D, Hq * Dh) * (D ** -0.5)),
            "wo": _w((lambda *s: torch.randn(*s, dtype=torch.bfloat16) * 0.2)(Hq * Dh, D) * ((Hq * Dh) ** -0.5)),
        }
    _A_in = _A_setup()

    def _B_setup():
        B, S, D, F = 1, 2048, 640, 1728
        return {
            "x": _w((lambda *s: torch.randn(*s, dtype=torch.bfloat16) * 0.05)(B, S, D)),
            "w_gate": _w((lambda *s: torch.randn(*s, dtype=torch.bfloat16) * 0.2)(D, F)),
            "w_up": _w((lambda *s: torch.randn(*s, dtype=torch.bfloat16) * 0.1)(D, F)),
            "w_down": _w((lambda *s: torch.randn(*s, dtype=torch.bfloat16) * 0.1)(F, D)),
        }
    _B_in = _B_setup()

    return {
        "A_x": _A_in["x"],
        "A_k_cache": _A_in["k_cache"],
        "A_v_cache": _A_in["v_cache"],
        "A_wq": _A_in["wq"],
        "A_wo": _A_in["wo"],
        "B_x": _B_in["x"],
        "B_w_gate": _B_in["w_gate"],
        "B_w_up": _B_in["w_up"],
        "B_w_down": _B_in["w_down"],
    }

def reference(A_x, A_k_cache, A_v_cache, A_wq, A_wo, B_x, B_w_gate, B_w_up, B_w_down):
    def _A_ref(x, k_cache, v_cache, wq, wo):
        B, Sq, D = x.shape
        Hq, Dh = 32, 128
        Hkv, Sk = k_cache.shape[1], k_cache.shape[2]
        W = min(4096, max(256, Sk // 4))
        rep = Hq // Hkv
        q = (lambda x, *d: x.permute(*d))((x @ wq).reshape(B, Sq, Hq, Dh), 0, 2, 1, 3)
        k = (lambda x, n, axis: torch.repeat_interleave(x, n, dim=axis))(k_cache[:, :, Sk - W:, :], rep, axis=1)
        v = (lambda x, n, axis: torch.repeat_interleave(x, n, dim=axis))(v_cache[:, :, Sk - W:, :], rep, axis=1)
        scores = torch.einsum("bhqd,bhkd->bhqk", q, k) * (Dh ** -0.5)
        p = _softmax(scores, axis=-1)
        o = torch.einsum("bhqk,bhkd->bhqd", p, v)
        return (lambda x, *d: x.permute(*d))(o, 0, 2, 1, 3).reshape(B, Sq, Hq * Dh) @ wo

    def _B_ref(x, w_gate, w_up, w_down):
        g = torch.einsum("bsd,df->bsf", x, w_gate)
        u = torch.einsum("bsd,df->bsf", x, w_up)
        h = _sigmoid(g) * u
        return torch.einsum("bsf,fd->bsd", h, w_down)

    x = A_x
    xn = _layernorm(x, axis=-1)
    s = _A_ref(xn, A_k_cache, A_v_cache, A_wq, A_wo)
    g = _silu(_B_ref(xn, B_w_gate, B_w_up, B_w_down))
    return x + s * g
```

```cpp
#include <hip/hip_runtime.h>
#include <hip/hip_bf16.h>
typedef unsigned short u16;
__device__ __forceinline__ float b2f(u16 v){ return __uint_as_float(((unsigned)v)<<16); }
__device__ __forceinline__ u16 f2b(float f){ unsigned u=__float_as_uint(f); u += 0x7fffu + ((u>>16)&1u); return (u16)(u>>16); }

__global__ void k_ln(const u16* x, u16* xn){
  int row = blockIdx.x; int t = threadIdx.x;
  float v[10]; float s=0;
  for(int i=0;i<10;i++){ v[i]=b2f(x[row*640+i*64+t]); s+=v[i]; }
  for(int o=32;o>0;o>>=1) s+=__shfl_xor(s,o);
  float m=s/640.f; float q=0;
  for(int i=0;i<10;i++){ float d=v[i]-m; q+=d*d; }
  for(int o=32;o>0;o>>=1) q+=__shfl_xor(q,o);
  float r=1.f/sqrtf(q/640.f+1e-5f);
  for(int i=0;i<10;i++) xn[row*640+i*64+t]=f2b((v[i]-m)*r);
}
__global__ void k_gemm(const u16* A,const u16* B,u16* C,int M,int N,int K){
  int n = blockIdx.x*256+threadIdx.x; int m0 = blockIdx.y*8;
  if(n>=N) return;
  float acc[8]={0,0,0,0,0,0,0,0};
  for(int k=0;k<K;k++){ float b=b2f(B[(long)k*N+n]);
    #pragma unroll
    for(int i=0;i<8;i++) acc[i]+=b2f(A[(long)(m0+i)*K+k])*b; }
  for(int i=0;i<8;i++) C[(long)(m0+i)*N+n]=f2b(acc[i]);
}
__global__ void k_attn(const u16* q,const u16* kc,const u16* vc,u16* o){
  __shared__ float sq[4][128]; __shared__ float sp[4][512];
  int h=blockIdx.y; int w=threadIdx.x>>6, l=threadIdx.x&63; int row=blockIdx.x*4+w;
  sq[w][l]=b2f(q[(long)row*4096+h*128+l]); sq[w][l+64]=b2f(q[(long)row*4096+h*128+l+64]);
  __syncthreads();
  const u16* K=kc+((long)h*2048+1536)*128; const u16* V=vc+((long)h*2048+1536)*128;
  float s[8]; float mx=-1e30f;
  for(int i=0;i<8;i++){ int key=i*64+l; float a=0; for(int d=0;d<128;d++) a+=sq[w][d]*b2f(K[key*128+d]); a*=0.08838834764831845f; s[i]=a; mx=fmaxf(mx,a);}
  for(int of=32;of>0;of>>=1) mx=fmaxf(mx,__shfl_xor(mx,of));
  float sum=0; for(int i=0;i<8;i++){ s[i]=__expf(s[i]-mx); sum+=s[i]; }
  for(int of=32;of>0;of>>=1) sum+=__shfl_xor(sum,of);
  for(int i=0;i<8;i++) sp[w][i*64+l]=s[i]/sum;
  __syncthreads();
  float a0=0,a1=0; for(int key=0;key<512;key++){ float p=sp[w][key]; a0+=p*b2f(V[key*128+l]); a1+=p*b2f(V[key*128+l+64]); }
  o[(long)row*4096+h*128+l]=f2b(a0); o[(long)row*4096+h*128+l+64]=f2b(a1);
}
__global__ void k_glu(const u16* g,const u16* u,u16* h,int n){ int i=blockIdx.x*256+threadIdx.x; if(i<n){ float gg=b2f(g[i]); h[i]=f2b(b2f(u[i])/(1.f+__expf(-gg))); } }
__global__ void k_fin(const u16* x,const u16* s,const u16* d,u16* out,int n){ int i=blockIdx.x*256+threadIdx.x; if(i<n){ float dd=b2f(d[i]); float g=dd/(1.f+__expf(-dd)); out[i]=f2b(b2f(x[i])+b2f(s[i])*g);} }

extern "C" void kernel_launch(void* const* d_in, const int* in_sizes, int n_in, void* d_out, int out_size, void* d_ws, size_t ws_size, hipStream_t stream) {
  const u16* x=(const u16*)d_in[0]; const u16* kc=(const u16*)d_in[1]; const u16* vc=(const u16*)d_in[2];
  const u16* wq=(const u16*)d_in[3]; const u16* wo=(const u16*)d_in[4];
  const u16* wg=(const u16*)d_in[6]; const u16* wu=(const u16*)d_in[7]; const u16* wd=(const u16*)d_in[8];
  u16* ws=(u16*)d_ws; u16* xn=ws; u16* q=xn+2048*640; u16* o=q+2048*4096; u16* s=o+2048*4096; u16* g=s+2048*640; u16* u=g+2048*1728; u16* h=u+2048*1728; u16* d=h+2048*1728;
  k_ln<<<2048,64,0,stream>>>(x,xn);
  k_gemm<<<dim3(16,256),256,0,stream>>>(xn,wq,q,2048,4096,640);
  k_attn<<<dim3(512,32),256,0,stream>>>(q,kc,vc,o);
  k_gemm<<<dim3(3,256),256,0,stream>>>(o,wo,s,2048,640,4096);
  k_gemm<<<dim3(7,256),256,0,stream>>>(xn,wg,g,2048,1728,640);
  k_gemm<<<dim3(7,256),256,0,stream>>>(xn,wu,u,2048,1728,640);
  k_glu<<<(2048*1728+255)/256,256,0,stream>>>(g,u,h,2048*1728);
  k_gemm<<<dim3(3,256),256,0,stream>>>(h,wd,d,2048,640,1728);
  k_fin<<<(2048*640+255)/256,256,0,stream>>>(x,s,d,(u16*)d_out,2048*640);
}
```

```cpp
#include <hip/hip_runtime.h>
#include <hip/hip_bf16.h>
typedef unsigned short u16;
typedef unsigned int u32;
__device__ __forceinline__ float b2f(u16 v){ return __uint_as_float(((unsigned)v)<<16); }
__device__ __forceinline__ u16 f2b(float f){ unsigned u=__float_as_uint(f); u += 0x7fffu + ((u>>16)&1u); return (u16)(u>>16); }
#define WS_XNF 0
#define WS_ACT (4u<<20)
#define WS_PART (32u<<20)
template<int N> __device__ __forceinline__ float dpp_ror(float v){ return __int_as_float(__builtin_amdgcn_update_dpp(0,__float_as_int(v),0x120+N,0xf,0xf,false)); }
__device__ __forceinline__ float row16_sum(float v){ v+=dpp_ror<8>(v); v+=dpp_ror<4>(v); v+=dpp_ror<2>(v); v+=dpp_ror<1>(v); return v; }
typedef u32 u32x4 __attribute__((ext_vector_type(4)));
__device__ __forceinline__ void st16(char* p,u32 a,u32 b,u32 c,u32 d){ u32x4 v={a,b,c,d};
  asm volatile("global_store_dwordx4 %0, %1, off sc1\n s_nop 1" :: "v"(p),"v"(v) : "memory"); }
__global__ __launch_bounds__(256) void k0_ln(const u16* __restrict__ x, const uint4* __restrict__ wq, char* __restrict__ ws){
  __shared__ u16 sx[16][648];
  const int t=threadIdx.x, R=blockIdx.x;
  if(R>=128){
    const uint4* p=wq+(size_t)(R-128)*2560+t; u32 a=0;
    #pragma unroll
    for(int i=0;i<10;i++){ uint4 v=p[i*256]; a^=v.x^v.y^v.z^v.w; }
    asm volatile("" :: "v"(a));
    return;
  }
  u32x4 cp={0,0,0,0};
  if(R<9){
    const char* pc; asm volatile("s_getpc_b64 %0" : "=s"(pc));
    const char* q=(const char*)((size_t)pc&~(size_t)15)+R*4096+t*16;
    asm volatile("global_load_dwordx4 %0, %1, off" : "=v"(cp) : "v"(q)); }
  const int row=t>>4, c=t&15;
  const uint4* p=(const uint4*)(x+(size_t)(R*16+row)*640);
  uint4 u[5];
  #pragma unroll
  for(int i=0;i<5;i++) u[i]=p[c+16*i];
  float v[40]; float s=0;
  #pragma unroll
  for(int i=0;i<5;i++){ const u32 w[4]={u[i].x,u[i].y,u[i].z,u[i].w};
    #pragma unroll
    for(int k=0;k<4;k++){ v[i*8+2*k]=__uint_as_float(w[k]<<16); v[i*8+2*k+1]=__uint_as_float(w[k]&0xffff0000u); s+=v[i*8+2*k]+v[i*8+2*k+1]; } }
  const float m=row16_sum(s)*(1.f/640.f); float q=0;
  #pragma unroll
  for(int k=0;k<40;k++){ v[k]-=m; q+=v[k]*v[k]; }
  const float r=1.f/sqrtf(row16_sum(q)*(1.f/640.f)+1e-5f);
  #pragma unroll
  for(int i=0;i<5;i++){ u32 o[4];
    #pragma unroll
    for(int k=0;k<4;k++){ const float lo=v[i*8+2*k]*r, hi=v[i*8+2*k+1]*r; asm("v_cvt_pk_bf16_f32 %0, %1, %2" : "=v"(o[k]) : "v"(lo),"v"(hi)); }
    *(uint4*)&sx[row][(c+16*i)*8]=make_uint4(o[0],o[1],o[2],o[3]); }
  __syncthreads();
  const int w=t>>6,l=t&63,rr=l&15,g=l>>4;
  #pragma unroll
  for(int i=0;i<5;i++){ const int st=i*4+w;
    uint2 a=*(const uint2*)&sx[rr][st*32+4*g], b=*(const uint2*)&sx[rr][st*32+16+4*g];
    st16(ws+WS_XNF+((size_t)(R*20+st)*64+l)*16,a.x,a.y,b.x,b.y); }
  if(t<128) st16(ws+WS_ACT+((size_t)(R*184+182)*64)*16+t*16,0,0,0,0);
  asm volatile("s_waitcnt vmcnt(0)" ::: "memory"); asm volatile("" :: "v"(cp));
}
__global__ __launch_bounds__(512,2) void k2_out(char* ws,const u16* wo,const u16* wd,const u16* x,u16* out){
  asm volatile(
    "s_mov_b64 s[72:73], %0\n"
    "s_mov_b64 s[42:43], %1\n"
    "s_mov_b64 s[44:45], %2\n"
    "s_mov_b64 s[46:47], %3\n"
    "s_mov_b64 s[48:49], %4\n"
    "s_mov_b64 s[74:75], %5\n"
    "s_mov_b32 s54, %6\n"
    "v_mov_b32 v1, %7\n"
    "v_lshrrev_b32 v2, 6, v1\n"
    "s_nop 0\n"
    "v_readfirstlane_b32 s33, v2\n"
    "v_and_b32 v1, 63, v1\n"
    "v_lshlrev_b32 v2, 4, v1\n"
    "s_getpc_b64 s[96:97]\n"
    "s_mul_i32 s79, s33, 1024\n"
    "s_add_u32 s96, s96, s79\n"
    "s_addc_u32 s97, s97, 0\n"
    "global_load_dwordx4 v[112:115], v2, s[96:97] offset:0\n"
    "s_and_b32 s55, s33, 3\n"
    "s_lshr_b32 s34, s33, 2\n"
    "s_and_b32 s56, s54, 7\n"
    "s_lshr_b32 s57, s54, 3\n"
    "s_mul_i32 s60, s57, 52\n"
    "s_lshr_b32 s60, s60, 8\n"
    "s_mul_i32 s56, s56, 6\n"
    "s_add_u32 s56, s56, s60\n"
    "s_mul_i32 s60, s60, 5\n"
    "s_sub_u32 s60, s57, s60\n"
    "s_lshr_b32 s58, s56, 4\n"
    "s_and_b32 s59, s56, 15\n"
    "s_lshl_b32 s61, s60, 8\n"
    "s_add_u32 s20, s42, s61\n"
    "s_addc_u32 s21, s43, 0\n"
    "s_mov_b32 s22, 2949120\n"
    "s_mov_b32 s28, 0\n"
    "s_mov_b32 s62, 18\n"
    "s_cmp_eq_u32 s58, 0\n"
    "s_cbranch_scc1 K2_R\n"
    "s_add_u32 s20, s20, 2949120\n"
    "s_addc_u32 s21, s21, 0\n"
    "s_mov_b32 s22, 2293760\n"
    "s_mov_b32 s28, 73728\n"
    "s_mov_b32 s62, 14\n"
    "s_cmp_eq_u32 s58, 1\n"
    "s_cbranch_scc1 K2_R\n"
    "s_add_u32 s20, s44, s61\n"
    "s_addc_u32 s21, s45, 0\n"
    "s_mov_b32 s22, 2211840\n"
    "s_mov_b32 s28, 131072\n"
    "s_mov_b32 s62, 14\n"
    "K2_R:\n"
    "s_sub_u32 s22, s22, s61\n"
    "s_mov_b32 s23, 0x20000\n"
    "s_add_u32 s24, s20, 128\n"
    "s_addc_u32 s25, s21, 0\n"
    "s_sub_u32 s26, s22, 128\n"
    "s_mov_b32 s27, 0x20000\n"
    "s_mul_i32 s50, s55, 1056\n"
    "s_mul_i32 s79, s34, 8448\n"
    "s_add_u32 s50, s50, s79\n"
    "s_add_u32 s64, s50, 0\n"
    "s_add_u32 s65, s50, 16896\n"
    "s_add_u32 s66, s50, 4224\n"
    "s_add_u32 s67, s50, 21120\n"
    "s_mov_b32 s51, 33792\n"
    "s_mov_b32 s76, 0\n"
    "s_mov_b32 s77, s51\n"
    "s_mov_b32 s40, 0\n"
    "s_mov_b32 s30, 0\n"
    "v_lshrrev_b32 v20, 4, v1\n"
    "v_lshlrev_b32 v23, 3, v20\n"
    "v_lshrrev_b32 v20, 3, v1\n"
    "v_and_b32 v20, 1, v20\n"
    "s_lshl_b32 s78, s55, 1\n"
    "s_lshl_b32 s41, s34, 5\n"
    "s_add_u32 s78, s78, s41\n"
    "v_add3_u32 v23, v23, v20, s78\n"
    "v_and_b32 v24, 7, v1\n"
    "v_lshlrev_b32 v24, 4, v24\n"
    "v_lshlrev_b32 v2, 4, v1\n"
    "v_mul_u32_u24 v26, 1280, v23\n"
    "v_add_u32 v10, v26, v24\n"
    "v_add_u32 v11, 81920, v10\n"
    "s_add_u32 m0, s40, s64\n"
    "s_nop 0\n"
    "buffer_load_dwordx4 v10, s[20:23], s30 offen lds\n"
    "s_add_u32 m0, s40, s65\n"
    "s_nop 0\n"
    "buffer_load_dwordx4 v11, s[20:23], s30 offen lds\n"
    "s_add_u32 m0, s40, s66\n"
    "s_nop 0\n"
    "buffer_load_dwordx4 v10, s[24:27], s30 offen lds\n"
    "s_add_u32 m0, s40, s67\n"
    "s_nop 0\n"
    "buffer_load_dwordx4 v11, s[24:27], s30 offen lds\n"
    "s_add_u32 s30, s30, 163840\n"
    "s_add_u32 s40, s40, s51\n"
    "s_cmp_eq_u32 s40, 135168\n"
    "s_cselect_b32 s40, 0, s40\n"
    "s_lshl_b32 s63, s59, 3\n"
    "s_lshl_b32 s78, s55, 1\n"
    "s_add_u32 s63, s63, s78\n"
    "s_mul_i32 s78, s63, 188416\n"
    "s_lshl_b32 s79, s34, 10\n"
    "s_add_u32 s78, s78, s79\n"
    "s_add_u32 s36, s72, s78\n"
    "s_addc_u32 s37, s73, 0\n"
    "s_mov_b32 s38, 0x7fffffff\n"
    "s_mov_b32 s39, 0x20000\n"
    "s_mul_i32 s78, s34, 188416\n"
    "s_add_u32 s29, s28, 188416\n"
    "s_sub_u32 s29, s29, s78\n"
    "s_add_u32 s28, s28, s78\n"
    "s_mov_b32 s35, 0\n"
    "buffer_load_dwordx4 a[0:3], v2, s[36:39], s28 offen offset:0\n"
    "buffer_load_dwordx4 a[4:7], v2, s[36:39], s29 offen offset:0\n"
    "buffer_load_dwordx4 a[8:11], v2, s[36:39], s28 offen offset:2048\n"
    "buffer_load_dwordx4 a[12:15], v2, s[36:39], s29 offen offset:2048\n"
    "s_add_u32 s35, s35, 1\n"
    "s_add_u32 s28, s28, 4096\n"
    "s_add_u32 s29, s29, 4096\n"
    "s_cmp_ge_u32 s35, s62\n"
    "s_cselect_b32 s38, 0, s38\n"
    "v_accvgpr_write_b32 a96, 0\n"
    "v_accvgpr_write_b32 a97, 0\n"
    "v_accvgpr_write_b32 a98, 0\n"
    "v_accvgpr_write_b32 a99, 0\n"
    "s_nop 1\n"
    "v_mfma_f32_32x32x16_bf16 v[32:47], a[96:99], a[96:99], 0\n"
    "v_mfma_f32_32x32x16_bf16 v[48:63], a[96:99], a[96:99], 0\n"
    "v_mfma_f32_32x32x16_bf16 v[64:79], a[96:99], a[96:99], 0\n"
    "v_mfma_f32_32x32x16_bf16 v[80:95], a[96:99], a[96:99], 0\n"
    "v_lshrrev_b32 v20, 4, v1\n"
    "v_and_b32 v20, 1, v20\n"
    "v_lshrrev_b32 v21, 3, v1\n"
    "v_and_b32 v21, 1, v21\n"
    "v_lshl_add_u32 v20, v20, 1, v21\n"
    "v_mul_u32_u24 v20, 1056, v20\n"
    "v_lshrrev_b32 v21, 2, v1\n"
    "v_and_b32 v21, 1, v21\n"
    "v_lshl_add_u32 v20, v21, 7, v20\n"
    "v_lshrrev_b32 v21, 5, v1\n"
    "v_lshl_add_u32 v20, v21, 8, v20\n"
    "v_and_b32 v21, 3, v1\n"
    "v_lshl_add_u32 v14, v21, 3, v20\n"
    "s_mul_i32 s79, s34, 8448\n"
    "v_add_u32 v14, s79, v14\n"
    "s_lshl_b32 s78, s59, 7\n"
    "s_lshl_b32 s79, s55, 5\n"
    "s_add_u32 s78, s78, s79\n"
    "s_lshl_b32 s79, s34, 4\n"
    "s_add_u32 s78, s78, s79\n"
    "s_mul_i32 s78, s78, 1280\n"
    "s_add_u32 s78, s78, s61\n"
    "s_add_u32 s80, s46, s78\n"
    "s_addc_u32 s81, s47, 0\n"
    "s_mov_b32 s82, 0x7fffffff\n"
    "s_mov_b32 s83, 0x20000\n"
    "s_add_u32 s84, s48, s78\n"
    "s_addc_u32 s85, s49, 0\n"
    "s_mov_b32 s86, 0x7fffffff\n"
    "s_mov_b32 s87, 0x20000\n"
    "v_and_b32 v4, 15, v1\n"
    "v_mul_u32_u24 v4, 1280, v4\n"
    "v_lshrrev_b32 v5, 4, v1\n"
    "v_and_b32 v6, 1, v5\n"
    "v_lshl_add_u32 v4, v6, 5, v4\n"
    "v_lshrrev_b32 v5, 1, v5\n"
    "v_lshl_add_u32 v4, v5, 4, v4\n"
    "s_add_u32 m0, s40, s64\n"
    "s_nop 0\n"
    "buffer_load_dwordx4 v10, s[20:23], s30 offen lds\n"
    "s_add_u32 m0, s40, s65\n"
    "s_nop 0\n"
    "buffer_load_dwordx4 v11, s[20:23], s30 offen lds\n"
    "s_add_u32 m0, s40, s66\n"
    "s_nop 0\n"
    "buffer_load_dwordx4 v10, s[24:27], s30 offen lds\n"
    "s_add_u32 m0, s40, s67\n"
    "s_nop 0\n"
    "buffer_load_dwordx4 v11, s[24:27], s30 offen lds\n"
    "s_add_u32 s30, s30, 163840\n"
    "s_add_u32 s40, s40, s51\n"
    "s_cmp_eq_u32 s40, 135168\n"
    "s_cselect_b32 s40, 0, s40\n"
    "buffer_load_dwordx4 a[16:19], v2, s[36:39], s28 offen offset:0\n"
    "buffer_load_dwordx4 a[20:23], v2, s[36:39], s29 offen offset:0\n"
    "buffer_load_dwordx4 a[24:27], v2, s[36:39], s28 offen offset:2048\n"
    "buffer_load_dwordx4 a[28:31], v2, s[36:39], s29 offen offset:2048\n"
    "s_add_u32 s35, s35, 1\n"
    "s_add_u32 s28, s28, 4096\n"
    "s_add_u32 s29, s29, 4096\n"
    "s_cmp_ge_u32 s35, s62\n"
    "s_cselect_b32 s38, 0, s38\n"
    "s_cmp_eq_u32 s58, 0\n"
    "s_cselect_b32 s82, 0x7fffffff, 0\n"
    "s_waitcnt vmcnt(12)\n"
    "s_barrier\n"
    "ds_read_b64_tr_b16 a[64:65], v14 offset:0\n"
    "ds_read_b64_tr_b16 a[66:67], v14 offset:512\n"
    "ds_read_b64_tr_b16 a[68:69], v14 offset:32\n"
    "ds_read_b64_tr_b16 a[70:71], v14 offset:544\n"
    "ds_read_b64_tr_b16 a[72:73], v14 offset:64\n"
    "ds_read_b64_tr_b16 a[74:75], v14 offset:576\n"
    "ds_read_b64_tr_b16 a[76:77], v14 offset:96\n"
    "ds_read_b64_tr_b16 a[78:79], v14 offset:608\n"
    "ds_read_b64_tr_b16 a[80:81], v14 offset:4224\n"
    "ds_read_b64_tr_b16 a[82:83], v14 offset:4736\n"
    "s_lshr_b32 s90, s62, 1\n"
    "s_sub_u32 s90, s90, 1\n"
    "s_mul_i32 s89, s59, 5\n"
    "s_add_u32 s89, s89, s60\n"
    "s_lshl_b32 s89, s89, 16\n"
    "s_lshl_b32 s79, s33, 12\n"
    "s_add_u32 s89, s89, s79\n"
    "s_add_u32 s68, s74, s89\n"
    "s_addc_u32 s69, s75, 0\n"
    "s_mov_b32 s70, 0x7fffffff\n"
    "s_mov_b32 s71, 0x20000\n"
    "s_mov_b32 s88, 32768\n"
    "K2_L:\n"
    "s_waitcnt lgkmcnt(8) vmcnt(11)\n"
    "v_mfma_f32_16x16x32_bf16 v[32:35], a[64:67], a[0:3], v[32:35]\n"
    "s_waitcnt vmcnt(10)\n"
    "ds_read_b64_tr_b16 a[84:85], v14 offset:4256\n"
    "ds_read_b64_tr_b16 a[86:87], v14 offset:4768\n"
    "ds_read_b64_tr_b16 a[88:89], v14 offset:4288\n"
    "ds_read_b64_tr_b16 a[90:91], v14 offset:4800\n"
    "v_mfma_f32_16x16x32_bf16 v[64:67], a[64:67], a[4:7], v[64:67]\n"
    "s_waitcnt lgkmcnt(10)\n"
    "s_add_u32 m0, s40, s64\n"
    "v_mfma_f32_16x16x32_bf16 v[36:39], a[68:71], a[0:3], v[36:39]\n"
    "ds_read_b64_tr_b16 a[92:93], v14 offset:4320\n"
    "ds_read_b64_tr_b16 a[94:95], v14 offset:4832\n"
    "buffer_load_dwordx4 v10, s[20:23], s30 offen lds\n"
    "v_mfma_f32_16x16x32_bf16 v[68:71], a[68:71], a[4:7], v[68:71]\n"
    "s_waitcnt lgkmcnt(10)\n"
    "v_mfma_f32_16x16x32_bf16 v[40:43], a[72:75], a[0:3], v[40:43]\n"
    "ds_read_b64_tr_b16 a[64:65], v14 offset:16896\n"
    "ds_read_b64_tr_b16 a[66:67], v14 offset:17408\n"
    "v_mfma_f32_16x16x32_bf16 v[72:75], a[72:75], a[4:7], v[72:75]\n"
    "s_waitcnt lgkmcnt(10)\n"
    "v_mfma_f32_16x16x32_bf16 v[44:47], a[76:79], a[0:3], v[44:47]\n"
    "s_add_u32 m0, s40, s65\n"
    "ds_read_b64_tr_b16 a[68:69], v14 offset:16928\n"
    "ds_read_b64_tr_b16 a[70:71], v14 offset:17440\n"
    "v_mfma_f32_16x16x32_bf16 v[76:79], a[76:79], a[4:7], v[76:79]\n"
    "s_waitcnt lgkmcnt(10)\n"
    "buffer_load_dwordx4 v11, s[20:23], s30 offen lds\n"
    "v_mfma_f32_16x16x32_bf16 v[48:51], a[80:83], a[0:3], v[48:51]\n"
    "ds_read_b64_tr_b16 a[72:73], v14 offset:16960\n"
    "ds_read_b64_tr_b16 a[74:75], v14 offset:17472\n"
    "v_mfma_f32_16x16x32_bf16 v[80:83], a[80:83], a[4:7], v[80:83]\n"
    "s_waitcnt lgkmcnt(10)\n"
    "v_mfma_f32_16x16x32_bf16 v[52:55], a[84:87], a[0:3], v[52:55]\n"
    "ds_read_b64_tr_b16 a[76:77], v14 offset:16992\n"
    "ds_read_b64_tr_b16 a[78:79], v14 offset:17504\n"
    "v_mfma_f32_16x16x32_bf16 v[84:87], a[84:87], a[4:7], v[84:87]\n"
    "s_waitcnt lgkmcnt(10)\n"
    "s_add_u32 m0, s40, s66\n"
    "v_mfma_f32_16x16x32_bf16 v[56:59], a[88:91], a[0:3], v[56:59]\n"
    "ds_read_b64_tr_b16 a[80:81], v14 offset:21120\n"
    "ds_read_b64_tr_b16 a[82:83], v14 offset:21632\n"
    "buffer_load_dwordx4 v10, s[24:27], s30 offen lds\n"
    "v_mfma_f32_16x16x32_bf16 v[88:91], a[88:91], a[4:7], v[88:91]\n"
    "s_waitcnt lgkmcnt(10)\n"
    "v_mfma_f32_16x16x32_bf16 v[60:63], a[92:95], a[0:3], v[60:63]\n"
    "ds_read_b64_tr_b16 a[84:85], v14 offset:21152\n"
    "ds_read_b64_tr_b16 a[86:87], v14 offset:21664\n"
    "v_mfma_f32_16x16x32_bf16 v[92:95], a[92:95], a[4:7], v[92:95]\n"
    "s_waitcnt lgkmcnt(10) vmcnt(11)\n"
    "buffer_load_dwordx4 a[0:3], v2, s[36:39], s28 offen offset:0\n"
    "v_mfma_f32_16x16x32_bf16 v[32:35], a[64:67], a[8:11], v[32:35]\n"
    "s_add_u32 m0, s40, s67\n"
    "ds_read_b64_tr_b16 a[88:89], v14 offset:21184\n"
    "ds_read_b64_tr_b16 a[90:91], v14 offset:21696\n"
    "buffer_load_dwordx4 a[4:7], v2, s[36:39], s29 offen offset:0\n"
    "v_mfma_f32_16x16x32_bf16 v[64:67], a[64:67], a[12:15], v[64:67]\n"
    "s_waitcnt lgkmcnt(10)\n"
    "buffer_load_dwordx4 v11, s[24:27], s30 offen lds\n"
    "v_mfma_f32_16x16x32_bf16 v[36:39], a[68:71], a[8:11], v[36:39]\n"
    "ds_read_b64_tr_b16 a[92:93], v14 offset:21216\n"
    "ds_read_b64_tr_b16 a[94:95], v14 offset:21728\n"
    "v_mfma_f32_16x16x32_bf16 v[68:71], a[68:71], a[12:15], v[68:71]\n"
    "s_waitcnt lgkmcnt(10)\n"
    "v_mfma_f32_16x16x32_bf16 v[40:43], a[72:75], a[8:11], v[40:43]\n"
    "s_waitcnt vmcnt(8)\n"
    "s_barrier\n"
    "v_add_u32 v14, s77, v14\n"
    "v_mfma_f32_16x16x32_bf16 v[72:75], a[72:75], a[12:15], v[72:75]\n"
    "s_waitcnt lgkmcnt(8)\n"
    "s_add_u32 s30, s30, 163840\n"
    "s_add_u32 s40, s40, s51\n"
    "ds_read_b64_tr_b16 a[64:65], v14 offset:0\n"
    "ds_read_b64_tr_b16 a[66:67], v14 offset:512\n"
    "v_mfma_f32_16x16x32_bf16 v[44:47], a[76:79], a[8:11], v[44:47]\n"
    "s_cmp_eq_u32 s40, 135168\n"
    "s_cselect_b32 s40, 0, s40\n"
    "ds_read_b64_tr_b16 a[68:69], v14 offset:32\n"
    "ds_read_b64_tr_b16 a[70:71], v14 offset:544\n"
    "v_mfma_f32_16x16x32_bf16 v[76:79], a[76:79], a[12:15], v[76:79]\n"
    "s_waitcnt lgkmcnt(10)\n"
    "s_add_u32 s76, s76, s77\n"
    "s_cmp_eq_u32 s76, 101376\n"
    "s_cselect_b32 s77, -101376, s51\n"
    "v_mfma_f32_16x16x32_bf16 v[48:51], a[80:83], a[8:11], v[48:51]\n"
    "ds_read_b64_tr_b16 a[72:73], v14 offset:64\n"
    "ds_read_b64_tr_b16 a[74:75], v14 offset:576\n"
    "v_mfma_f32_16x16x32_bf16 v[80:83], a[80:83], a[12:15], v[80:83]\n"
    "s_waitcnt lgkmcnt(10)\n"
    "v_mfma_f32_16x16x32_bf16 v[52:55], a[84:87], a[8:11], v[52:55]\n"
    "ds_read_b64_tr_b16 a[76:77], v14 offset:96\n"
    "ds_read_b64_tr_b16 a[78:79], v14 offset:608\n"
    "v_mfma_f32_16x16x32_bf16 v[84:87], a[84:87], a[12:15], v[84:87]\n"
    "s_waitcnt lgkmcnt(10)\n"
    "v_mfma_f32_16x16x32_bf16 v[56:59], a[88:91], a[8:11], v[56:59]\n"
    "ds_read_b64_tr_b16 a[80:81], v14 offset:4224\n"
    "ds_read_b64_tr_b16 a[82:83], v14 offset:4736\n"
    "v_mfma_f32_16x16x32_bf16 v[88:91], a[88:91], a[12:15], v[88:91]\n"
    "s_waitcnt lgkmcnt(10)\n"
    "v_mfma_f32_16x16x32_bf16 v[60:63], a[92:95], a[8:11], v[60:63]\n"
    "ds_read_b64_tr_b16 a[84:85], v14 offset:4256\n"
    "ds_read_b64_tr_b16 a[86:87], v14 offset:4768\n"
    "v_mfma_f32_16x16x32_bf16 v[92:95], a[92:95], a[12:15], v[92:95]\n"
    "s_waitcnt lgkmcnt(10)\n"
    "buffer_load_dwordx4 a[8:11], v2, s[36:39], s28 offen offset:2048\n"
    "v_mfma_f32_16x16x32_bf16 v[32:35], a[64:67], a[16:19], v[32:35]\n"
    "ds_read_b64_tr_b16 a[88:89], v14 offset:4288\n"
    "ds_read_b64_tr_b16 a[90:91], v14 offset:4800\n"
    "buffer_load_dwordx4 a[12:15], v2, s[36:39], s29 offen offset:2048\n"
    "v_mfma_f32_16x16x32_bf16 v[64:67], a[64:67], a[20:23], v[64:67]\n"
    "s_waitcnt lgkmcnt(10)\n"
    "s_add_u32 m0, s40, s64\n"
    "s_add_u32 s35, s35, 1\n"
    "s_add_u32 s28, s28, 4096\n"
    "s_add_u32 s29, s29, 4096\n"
    "v_mfma_f32_16x16x32_bf16 v[36:39], a[68:71], a[16:19], v[36:39]\n"
    "ds_read_b64_tr_b16 a[92:93], v14 offset:4320\n"
    "ds_read_b64_tr_b16 a[94:95], v14 offset:4832\n"
    "buffer_load_dwordx4 v10, s[20:23], s30 offen lds\n"
    "v_mfma_f32_16x16x32_bf16 v[68:71], a[68:71], a[20:23], v[68:71]\n"
    "s_waitcnt lgkmcnt(10)\n"
    "s_cmp_ge_u32 s35, s62\n"
    "s_cselect_b32 s38, 0, s38\n"
    "v_mfma_f32_16x16x32_bf16 v[40:43], a[72:75], a[16:19], v[40:43]\n"
    "ds_read_b64_tr_b16 a[64:65], v14 offset:16896\n"
    "ds_read_b64_tr_b16 a[66:67], v14 offset:17408\n"
    "v_mfma_f32_16x16x32_bf16 v[72:75], a[72:75], a[20:23], v[72:75]\n"
    "s_waitcnt lgkmcnt(10)\n"
    "v_mfma_f32_16x16x32_bf16 v[44:47], a[76:79], a[16:19], v[44:47]\n"
    "s_add_u32 m0, s40, s65\n"
    "ds_read_b64_tr_b16 a[68:69], v14 offset:16928\n"
    "ds_read_b64_tr_b16 a[70:71], v14 offset:17440\n"
    "v_mfma_f32_16x16x32_bf16 v[76:79], a[76:79], a[20:23], v[76:79]\n"
    "s_waitcnt lgkmcnt(10)\n"
    "buffer_load_dwordx4 v11, s[20:23], s30 offen lds\n"
    "v_mfma_f32_16x16x32_bf16 v[48:51], a[80:83], a[16:19], v[48:51]\n"
    "ds_read_b64_tr_b16 a[72:73], v14 offset:16960\n"
    "ds_read_b64_tr_b16 a[74:75], v14 offset:17472\n"
    "v_mfma_f32_16x16x32_bf16 v[80:83], a[80:83], a[20:23], v[80:83]\n"
    "s_waitcnt lgkmcnt(10)\n"
    "v_mfma_f32_16x16x32_bf16 v[52:55], a[84:87], a[16:19], v[52:55]\n"
    "ds_read_b64_tr_b16 a[76:77], v14 offset:16992\n"
    "ds_read_b64_tr_b16 a[78:79], v14 offset:17504\n"
    "v_mfma_f32_16x16x32_bf16 v[84:87], a[84:87], a[20:23], v[84:87]\n"
    "s_waitcnt lgkmcnt(10)\n"
    "s_add_u32 m0, s40, s66\n"
    "v_mfma_f32_16x16x32_bf16 v[56:59], a[88:91], a[16:19], v[56:59]\n"
    "ds_read_b64_tr_b16 a[80:81], v14 offset:21120\n"
    "ds_read_b64_tr_b16 a[82:83], v14 offset:21632\n"
    "buffer_load_dwordx4 v10, s[24:27], s30 offen lds\n"
    "v_mfma_f32_16x16x32_bf16 v[88:91], a[88:91], a[20:23], v[88:91]\n"
    "s_waitcnt lgkmcnt(10)\n"
    "v_mfma_f32_16x16x32_bf16 v[60:63], a[92:95], a[16:19], v[60:63]\n"
    "ds_read_b64_tr_b16 a[84:85], v14 offset:21152\n"
    "ds_read_b64_tr_b16 a[86:87], v14 offset:21664\n"
    "v_mfma_f32_16x16x32_bf16 v[92:95], a[92:95], a[20:23], v[92:95]\n"
    "s_waitcnt lgkmcnt(10) vmcnt(11)\n"
    "buffer_load_dwordx4 a[16:19], v2, s[36:39], s28 offen offset:0\n"
    "v_mfma_f32_16x16x32_bf16 v[32:35], a[64:67], a[24:27], v[32:35]\n"
    "s_add_u32 m0, s40, s67\n"
    "ds_read_b64_tr_b16 a[88:89], v14 offset:21184\n"
    "ds_read_b64_tr_b16 a[90:91], v14 offset:21696\n"
    "buffer_load_dwordx4 a[20:23], v2, s[36:39], s29 offen offset:0\n"
    "v_mfma_f32_16x16x32_bf16 v[64:67], a[64:67], a[28:31], v[64:67]\n"
    "s_waitcnt lgkmcnt(10)\n"
    "buffer_load_dwordx4 v11, s[24:27], s30 offen lds\n"
    "v_mfma_f32_16x16x32_bf16 v[36:39], a[68:71], a[24:27], v[36:39]\n"
    "ds_read_b64_tr_b16 a[92:93], v14 offset:21216\n"
    "ds_read_b64_tr_b16 a[94:95], v14 offset:21728\n"
    "v_mfma_f32_16x16x32_bf16 v[68:71], a[68:71], a[28:31], v[68:71]\n"
    "s_waitcnt lgkmcnt(10)\n"
    "v_mfma_f32_16x16x32_bf16 v[40:43], a[72:75], a[24:27], v[40:43]\n"
    "s_waitcnt vmcnt(8)\n"
    "s_barrier\n"
    "v_add_u32 v14, s77, v14\n"
    "v_mfma_f32_16x16x32_bf16 v[72:75], a[72:75], a[28:31], v[72:75]\n"
    "s_waitcnt lgkmcnt(8)\n"
    "s_add_u32 s30, s30, 163840\n"
    "s_add_u32 s40, s40, s51\n"
    "ds_read_b64_tr_b16 a[64:65], v14 offset:0\n"
    "ds_read_b64_tr_b16 a[66:67], v14 offset:512\n"
    "v_mfma_f32_16x16x32_bf16 v[44:47], a[76:79], a[24:27], v[44:47]\n"
    "s_cmp_eq_u32 s40, 135168\n"
    "s_cselect_b32 s40, 0, s40\n"
    "ds_read_b64_tr_b16 a[68:69], v14 offset:32\n"
    "ds_read_b64_tr_b16 a[70:71], v14 offset:544\n"
    "v_mfma_f32_16x16x32_bf16 v[76:79], a[76:79], a[28:31], v[76:79]\n"
    "s_waitcnt lgkmcnt(10)\n"
    "s_add_u32 s76, s76, s77\n"
    "s_cmp_eq_u32 s76, 101376\n"
    "s_cselect_b32 s77, -101376, s51\n"
    "v_mfma_f32_16x16x32_bf16 v[48:51], a[80:83], a[24:27], v[48:51]\n"
    "ds_read_b64_tr_b16 a[72:73], v14 offset:64\n"
    "ds_read_b64_tr_b16 a[74:75], v14 offset:576\n"
    "v_mfma_f32_16x16x32_bf16 v[80:83], a[80:83], a[28:31], v[80:83]\n"
    "s_waitcnt lgkmcnt(10)\n"
    "v_mfma_f32_16x16x32_bf16 v[52:55], a[84:87], a[24:27], v[52:55]\n"
    "ds_read_b64_tr_b16 a[76:77], v14 offset:96\n"
    "ds_read_b64_tr_b16 a[78:79], v14 offset:608\n"
    "v_mfma_f32_16x16x32_bf16 v[84:87], a[84:87], a[28:31], v[84:87]\n"
    "s_waitcnt lgkmcnt(10)\n"
    "v_mfma_f32_16x16x32_bf16 v[56:59], a[88:91], a[24:27], v[56:59]\n"
    "ds_read_b64_tr_b16 a[80:81], v14 offset:4224\n"
    "ds_read_b64_tr_b16 a[82:83], v14 offset:4736\n"
    "v_mfma_f32_16x16x32_bf16 v[88:91], a[88:91], a[28:31], v[88:91]\n"
    "s_waitcnt lgkmcnt(10)\n"
    "v_mfma_f32_16x16x32_bf16 v[60:63], a[92:95], a[24:27], v[60:63]\n"
    "v_mfma_f32_16x16x32_bf16 v[92:95], a[92:95], a[28:31], v[92:95]\n"
    "buffer_load_dwordx4 a[24:27], v2, s[36:39], s28 offen offset:2048\n"
    "buffer_load_dwordx4 a[28:31], v2, s[36:39], s29 offen offset:2048\n"
    "s_add_u32 s35, s35, 1\n"
    "s_add_u32 s28, s28, 4096\n"
    "s_add_u32 s29, s29, 4096\n"
    "s_cmp_ge_u32 s35, s62\n"
    "s_cselect_b32 s38, 0, s38\n"
    "s_sub_u32 s90, s90, 1\n"
    "s_cmp_lg_u32 s90, 0\n"
    "s_cbranch_scc1 K2_L\n"
    "s_cmp_eq_u32 s58, 0\n"
    "s_cselect_b32 s70, 0x7fffffff, 0\n"
    "s_mov_b32 s82, s70\n"
    "s_lshl_b32 s78, s33, 13\n"
    "s_add_u32 s78, s78, s40\n"
    "v_add_u32 v6, s78, v2\n"
    "s_lshl_b32 s79, s33, 13\n"
    "s_xor_b32 s79, s79, 32768\n"
    "s_add_u32 s79, s79, s40\n"
    "v_add_u32 v7, s79, v2\n"
    "s_getpc_b64 s[92:93]\n"
    "s_lshl_b32 s79, s33, 10\n"
    "s_add_u32 s92, s92, s79\n"
    "s_addc_u32 s93, s93, 0\n"
    "global_load_dwordx4 v[112:115], v2, s[92:93]\n"
    "s_waitcnt lgkmcnt(8)\n"
    "v_mfma_f32_16x16x32_bf16 v[32:35], a[64:67], a[0:3], v[32:35]\n"
    "ds_read_b64_tr_b16 a[84:85], v14 offset:4256\n"
    "ds_read_b64_tr_b16 a[86:87], v14 offset:4768\n"
    "ds_read_b64_tr_b16 a[88:89], v14 offset:4288\n"
    "ds_read_b64_tr_b16 a[90:91], v14 offset:4800\n"
    "v_mfma_f32_16x16x32_bf16 v[64:67], a[64:67], a[4:7], v[64:67]\n"
    "s_waitcnt lgkmcnt(10)\n"
    "buffer_load_dwordx4 v[96:99], v4, s[80:83], 0 offen offset:0\n"
    "v_mfma_f32_16x16x32_bf16 v[36:39], a[68:71], a[0:3], v[36:39]\n"
    "ds_read_b64_tr_b16 a[92:93], v14 offset:4320\n"
    "ds_read_b64_tr_b16 a[94:95], v14 offset:4832\n"
    "v_mfma_f32_16x16x32_bf16 v[68:71], a[68:71], a[4:7], v[68:71]\n"
    "s_waitcnt lgkmcnt(10)\n"
    "v_mfma_f32_16x16x32_bf16 v[40:43], a[72:75], a[0:3], v[40:43]\n"
    "ds_read_b64_tr_b16 a[64:65], v14 offset:16896\n"
    "ds_read_b64_tr_b16 a[66:67], v14 offset:17408\n"
    "v_mfma_f32_16x16x32_bf16 v[72:75], a[72:75], a[4:7], v[72:75]\n"
    "s_waitcnt lgkmcnt(10)\n"
    "v_mfma_f32_16x16x32_bf16 v[44:47], a[76:79], a[0:3], v[44:47]\n"
    "ds_read_b64_tr_b16 a[68:69], v14 offset:16928\n"
    "buffer_load_dwordx4 v[100:103], v4, s[80:83], 0 offen offset:64\n"
    "ds_read_b64_tr_b16 a[70:71], v14 offset:17440\n"
    "v_mfma_f32_16x16x32_bf16 v[76:79], a[76:79], a[4:7], v[76:79]\n"
    "s_waitcnt lgkmcnt(10)\n"
    "v_mfma_f32_16x16x32_bf16 v[48:51], a[80:83], a[0:3], v[48:51]\n"
    "ds_read_b64_tr_b16 a[72:73], v14 offset:16960\n"
    "ds_read_b64_tr_b16 a[74:75], v14 offset:17472\n"
    "v_mfma_f32_16x16x32_bf16 v[80:83], a[80:83], a[4:7], v[80:83]\n"
    "s_waitcnt lgkmcnt(10)\n"
    "v_mfma_f32_16x16x32_bf16 v[52:55], a[84:87], a[0:3], v[52:55]\n"
    "ds_read_b64_tr_b16 a[76:77], v14 offset:16992\n"
    "ds_read_b64_tr_b16 a[78:79], v14 offset:17504\n"
    "v_mfma_f32_16x16x32_bf16 v[84:87], a[84:87], a[4:7], v[84:87]\n"
    "s_waitcnt lgkmcnt(10)\n"
    "buffer_load_dwordx4 v[104:107], v4, s[80:83], 0 offen offset:128\n"
    "v_mfma_f32_16x16x32_bf16 v[56:59], a[88:91], a[0:3], v[56:59]\n"
    "ds_read_b64_tr_b16 a[80:81], v14 offset:21120\n"
    "ds_read_b64_tr_b16 a[82:83], v14 offset:21632\n"
    "v_mfma_f32_16x16x32_bf16 v[88:91], a[88:91], a[4:7], v[88:91]\n"
    "s_waitcnt lgkmcnt(10)\n"
    "v_mfma_f32_16x16x32_bf16 v[60:63], a[92:95], a[0:3], v[60:63]\n"
    "ds_read_b64_tr_b16 a[84:85], v14 offset:21152\n"
    "ds_read_b64_tr_b16 a[86:87], v14 offset:21664\n"
    "v_mfma_f32_16x16x32_bf16 v[92:95], a[92:95], a[4:7], v[92:95]\n"
    "s_waitcnt lgkmcnt(10) vmcnt(12)\n"
    "v_mfma_f32_16x16x32_bf16 v[32:35], a[64:67], a[8:11], v[32:35]\n"
    "ds_read_b64_tr_b16 a[88:89], v14 offset:21184\n"
    "buffer_load_dwordx4 v[108:111], v4, s[80:83], 0 offen offset:192\n"
    "ds_read_b64_tr_b16 a[90:91], v14 offset:21696\n"
    "v_mfma_f32_16x16x32_bf16 v[64:67], a[64:67], a[12:15], v[64:67]\n"
    "s_waitcnt lgkmcnt(10)\n"
    "v_mfma_f32_16x16x32_bf16 v[36:39], a[68:71], a[8:11], v[36:39]\n"
    "ds_read_b64_tr_b16 a[92:93], v14 offset:21216\n"
    "ds_read_b64_tr_b16 a[94:95], v14 offset:21728\n"
    "v_mfma_f32_16x16x32_bf16 v[68:71], a[68:71], a[12:15], v[68:71]\n"
    "s_waitcnt lgkmcnt(10)\n"
    "v_mfma_f32_16x16x32_bf16 v[40:43], a[72:75], a[8:11], v[40:43]\n"
    "s_waitcnt vmcnt(7)\n"
    "s_barrier\n"
    "v_add_u32 v14, s77, v14\n"
    "v_mfma_f32_16x16x32_bf16 v[72:75], a[72:75], a[12:15], v[72:75]\n"
    "s_waitcnt lgkmcnt(8)\n"
    "ds_read_b64_tr_b16 a[64:65], v14 offset:0\n"
    "ds_read_b64_tr_b16 a[66:67], v14 offset:512\n"
    "ds_read_b64_tr_b16 a[68:69], v14 offset:32\n"
    "ds_read_b64_tr_b16 a[70:71], v14 offset:544\n"
    "v_mfma_f32_16x16x32_bf16 v[44:47], a[76:79], a[8:11], v[44:47]\n"
    "ds_read_b64_tr_b16 a[72:73], v14 offset:64\n"
    "ds_read_b64_tr_b16 a[74:75], v14 offset:576\n"
    "v_mfma_f32_16x16x32_bf16 v[76:79], a[76:79], a[12:15], v[76:79]\n"
    "s_waitcnt lgkmcnt(12)\n"
    "v_mfma_f32_16x16x32_bf16 v[48:51], a[80:83], a[8:11], v[48:51]\n"
    "ds_read_b64_tr_b16 a[76:77], v14 offset:96\n"
    "ds_read_b64_tr_b16 a[78:79], v14 offset:608\n"
    "v_mfma_f32_16x16x32_bf16 v[80:83], a[80:83], a[12:15], v[80:83]\n"
    "s_waitcnt lgkmcnt(12)\n"
    "v_mfma_f32_16x16x32_bf16 v[52:55], a[84:87], a[8:11], v[52:55]\n"
    "ds_read_b64_tr_b16 a[80:81], v14 offset:4224\n"
    "ds_read_b64_tr_b16 a[82:83], v14 offset:4736\n"
    "ds_read_b64_tr_b16 a[96:97], v14 offset:16896\n"
    "ds_read_b64_tr_b16 a[98:99], v14 offset:17408\n"
    "v_mfma_f32_16x16x32_bf16 v[84:87], a[84:87], a[12:15], v[84:87]\n"
    "s_waitcnt lgkmcnt(14)\n"
    "v_mfma_f32_16x16x32_bf16 v[56:59], a[88:91], a[8:11], v[56:59]\n"
    "ds_read_b64_tr_b16 a[84:85], v14 offset:4256\n"
    "ds_read_b64_tr_b16 a[86:87], v14 offset:4768\n"
    "ds_read_b64_tr_b16 a[100:101], v14 offset:16928\n"
    "ds_read_b64_tr_b16 a[102:103], v14 offset:17440\n"
    "v_mfma_f32_16x16x32_bf16 v[88:91], a[88:91], a[12:15], v[88:91]\n"
    "s_waitcnt lgkmcnt(15)\n"
    "v_mfma_f32_16x16x32_bf16 v[60:63], a[92:95], a[8:11], v[60:63]\n"
    "ds_read_b64_tr_b16 a[88:89], v14 offset:4288\n"
    "ds_read_b64_tr_b16 a[90:91], v14 offset:4800\n"
    "ds_read_b64_tr_b16 a[104:105], v14 offset:16960\n"
    "ds_read_b64_tr_b16 a[106:107], v14 offset:17472\n"
    "v_mfma_f32_16x16x32_bf16 v[92:95], a[92:95], a[12:15], v[92:95]\n"
    "s_waitcnt lgkmcnt(15)\n"
    "v_mfma_f32_16x16x32_bf16 v[64:67], a[64:67], a[20:23], v[64:67]\n"
    "ds_read_b64_tr_b16 a[92:93], v14 offset:4320\n"
    "ds_read_b64_tr_b16 a[94:95], v14 offset:4832\n"
    "ds_read_b64_tr_b16 a[108:109], v14 offset:16992\n"
    "ds_read_b64_tr_b16 a[110:111], v14 offset:17504\n"
    "v_mfma_f32_16x16x32_bf16 v[68:71], a[68:71], a[20:23], v[68:71]\n"
    "s_waitcnt lgkmcnt(15)\n"
    "ds_read_b64_tr_b16 a[112:113], v14 offset:21120\n"
    "ds_read_b64_tr_b16 a[114:115], v14 offset:21632\n"
    "ds_read_b64_tr_b16 a[116:117], v14 offset:21152\n"
    "ds_read_b64_tr_b16 a[118:119], v14 offset:21664\n"
    "v_mfma_f32_16x16x32_bf16 v[72:75], a[72:75], a[20:23], v[72:75]\n"
    "s_waitcnt lgkmcnt(15)\n"
    "ds_read_b64_tr_b16 a[120:121], v14 offset:21184\n"
    "ds_read_b64_tr_b16 a[122:123], v14 offset:21696\n"
    "ds_read_b64_tr_b16 a[124:125], v14 offset:21216\n"
    "ds_read_b64_tr_b16 a[126:127], v14 offset:21728\n"
    "v_mfma_f32_16x16x32_bf16 v[76:79], a[76:79], a[20:23], v[76:79]\n"
    "s_waitcnt lgkmcnt(15)\n"
    "v_mfma_f32_16x16x32_bf16 v[80:83], a[80:83], a[20:23], v[80:83]\n"
    "v_mfma_f32_16x16x32_bf16 v[84:87], a[84:87], a[20:23], v[84:87]\n"
    "s_waitcnt lgkmcnt(14)\n"
    "v_mfma_f32_16x16x32_bf16 v[88:91], a[88:91], a[20:23], v[88:91]\n"
    "s_waitcnt lgkmcnt(10)\n"
    "v_mfma_f32_16x16x32_bf16 v[92:95], a[92:95], a[20:23], v[92:95]\n"
    "s_waitcnt vmcnt(5)\n"
    "v_mfma_f32_16x16x32_bf16 v[64:67], a[96:99], a[28:31], v[64:67]\n"
    "v_mfma_f32_16x16x32_bf16 v[68:71], a[100:103], a[28:31], v[68:71]\n"
    "v_mfma_f32_16x16x32_bf16 v[72:75], a[104:107], a[28:31], v[72:75]\n"
    "s_waitcnt lgkmcnt(8)\n"
    "v_mfma_f32_16x16x32_bf16 v[76:79], a[108:111], a[28:31], v[76:79]\n"
    "s_waitcnt lgkmcnt(6)\n"
    "v_mfma_f32_16x16x32_bf16 v[80:83], a[112:115], a[28:31], v[80:83]\n"
    "s_waitcnt lgkmcnt(4)\n"
    "s_nop 0\n"
    "ds_write_b128 v6, v[64:67] offset:0\n"
    "v_mfma_f32_16x16x32_bf16 v[84:87], a[116:119], a[28:31], v[84:87]\n"
    "s_waitcnt lgkmcnt(3)\n"
    "v_mfma_f32_16x16x32_bf16 v[88:91], a[120:123], a[28:31], v[88:91]\n"
    "s_waitcnt lgkmcnt(1)\n"
    "ds_write_b128 v6, v[68:71] offset:1024\n"
    "v_mfma_f32_16x16x32_bf16 v[92:95], a[124:127], a[28:31], v[92:95]\n"
    "v_mfma_f32_16x16x32_bf16 v[32:35], a[64:67], a[16:19], v[32:35]\n"
    "ds_write_b128 v6, v[72:75] offset:2048\n"
    "v_mfma_f32_16x16x32_bf16 v[36:39], a[68:71], a[16:19], v[36:39]\n"
    "v_mfma_f32_16x16x32_bf16 v[40:43], a[72:75], a[16:19], v[40:43]\n"
    "ds_write_b128 v6, v[76:79] offset:3072\n"
    "v_mfma_f32_16x16x32_bf16 v[44:47], a[76:79], a[16:19], v[44:47]\n"
    "v_mfma_f32_16x16x32_bf16 v[48:51], a[80:83], a[16:19], v[48:51]\n"
    "ds_write_b128 v6, v[80:83] offset:4096\n"
    "v_mfma_f32_16x16x32_bf16 v[52:55], a[84:87], a[16:19], v[52:55]\n"
    "v_mfma_f32_16x16x32_bf16 v[56:59], a[88:91], a[16:19], v[56:59]\n"
    "ds_write_b128 v6, v[84:87] offset:5120\n"
    "v_mfma_f32_16x16x32_bf16 v[60:63], a[92:95], a[16:19], v[60:63]\n"
    "buffer_load_dwordx4 a[48:51], v2, s[68:71], s88 offen offset:0 sc1\n"
    "v_mfma_f32_16x16x32_bf16 v[32:35], a[96:99], a[24:27], v[32:35]\n"
    "ds_write_b128 v6, v[88:91] offset:6144\n"
    "buffer_load_dwordx4 a[52:55], v2, s[68:71], s88 offen offset:1024 sc1\n"
    "v_mfma_f32_16x16x32_bf16 v[36:39], a[100:103], a[24:27], v[36:39]\n"
    "buffer_load_dwordx4 a[56:59], v2, s[68:71], s88 offen offset:2048 sc1\n"
    "v_mfma_f32_16x16x32_bf16 v[40:43], a[104:107], a[24:27], v[40:43]\n"
    "ds_write_b128 v6, v[92:95] offset:7168\n"
    "buffer_load_dwordx4 a[60:63], v2, s[68:71], s88 offen offset:3072 sc1\n"
    "v_mfma_f32_16x16x32_bf16 v[44:47], a[108:111], a[24:27], v[44:47]\n"
    "buffer_load_dwordx4 a[32:35], v2, s[68:71], 0 offen offset:0 sc1\n"
    "v_mfma_f32_16x16x32_bf16 v[48:51], a[112:115], a[24:27], v[48:51]\n"
    "buffer_load_dwordx4 a[36:39], v2, s[68:71], 0 offen offset:1024 sc1\n"
    "v_mfma_f32_16x16x32_bf16 v[52:55], a[116:119], a[24:27], v[52:55]\n"
    "buffer_load_dwordx4 a[40:43], v2, s[68:71], 0 offen offset:2048 sc1\n"
    "v_mfma_f32_16x16x32_bf16 v[56:59], a[120:123], a[24:27], v[56:59]\n"
    "buffer_load_dwordx4 a[44:47], v2, s[68:71], 0 offen offset:3072 sc1\n"
    "v_mfma_f32_16x16x32_bf16 v[60:63], a[124:127], a[24:27], v[60:63]\n"
    "s_mov_b32 s70, 0x7fffffff\n"
    "s_waitcnt lgkmcnt(0)\n"
    "s_barrier\n"
    "ds_read_b128 v[64:67], v7 offset:0\n"
    "ds_read_b128 v[68:71], v7 offset:1024\n"
    "ds_read_b128 v[72:75], v7 offset:2048\n"
    "ds_read_b128 v[76:79], v7 offset:3072\n"
    "ds_read_b128 v[80:83], v7 offset:4096\n"
    "ds_read_b128 v[84:87], v7 offset:5120\n"
    "ds_read_b128 v[88:91], v7 offset:6144\n"
    "ds_read_b128 v[92:95], v7 offset:7168\n"
    "s_waitcnt lgkmcnt(7)\n"
    "v_add_f32 v32, v32, v64\n"
    "v_add_f32 v33, v33, v65\n"
    "v_add_f32 v34, v34, v66\n"
    "v_add_f32 v35, v35, v67\n"
    "s_waitcnt lgkmcnt(6)\n"
    "v_add_f32 v36, v36, v68\n"
    "v_add_f32 v37, v37, v69\n"
    "v_add_f32 v38, v38, v70\n"
    "v_add_f32 v39, v39, v71\n"
    "s_waitcnt lgkmcnt(5)\n"
    "v_add_f32 v40, v40, v72\n"
    "v_add_f32 v41, v41, v73\n"
    "v_add_f32 v42, v42, v74\n"
    "v_add_f32 v43, v43, v75\n"
    "s_waitcnt lgkmcnt(4)\n"
    "v_add_f32 v44, v44, v76\n"
    "v_add_f32 v45, v45, v77\n"
    "v_add_f32 v46, v46, v78\n"
    "v_add_f32 v47, v47, v79\n"
    "s_waitcnt lgkmcnt(3)\n"
    "v_add_f32 v48, v48, v80\n"
    "v_add_f32 v49, v49, v81\n"
    "v_add_f32 v50, v50, v82\n"
    "v_add_f32 v51, v51, v83\n"
    "s_waitcnt lgkmcnt(2)\n"
    "v_add_f32 v52, v52, v84\n"
    "v_add_f32 v53, v53, v85\n"
    "v_add_f32 v54, v54, v86\n"
    "v_add_f32 v55, v55, v87\n"
    "s_waitcnt lgkmcnt(1)\n"
    "v_add_f32 v56, v56, v88\n"
    "v_add_f32 v57, v57, v89\n"
    "v_add_f32 v58, v58, v90\n"
    "v_add_f32 v59, v59, v91\n"
    "s_waitcnt lgkmcnt(0)\n"
    "v_add_f32 v60, v60, v92\n"
    "v_add_f32 v61, v61, v93\n"
    "v_add_f32 v62, v62, v94\n"
    "v_add_f32 v63, v63, v95\n"
    "s_cmp_eq_u32 s58, 0\n"
    "s_cbranch_scc1 K2_FIN\n"
    "s_cmp_eq_u32 s58, 1\n"
    "s_cbranch_scc1 K2_ST\n"
    "v_mul_f32 v64, 0xbfb8aa3b, v32\n"
    "v_mul_f32 v65, 0xbfb8aa3b, v33\n"
    "v_mul_f32 v66, 0xbfb8aa3b, v34\n"
    "v_mul_f32 v67, 0xbfb8aa3b, v35\n"
    "v_mul_f32 v68, 0xbfb8aa3b, v36\n"
    "v_mul_f32 v69, 0xbfb8aa3b, v37\n"
    "v_mul_f32 v70, 0xbfb8aa3b, v38\n"
    "v_mul_f32 v71, 0xbfb8aa3b, v39\n"
    "v_mul_f32 v72, 0xbfb8aa3b, v40\n"
    "v_mul_f32 v73, 0xbfb8aa3b, v41\n"
    "v_mul_f32 v74, 0xbfb8aa3b, v42\n"
    "v_mul_f32 v75, 0xbfb8aa3b, v43\n"
    "v_mul_f32 v76, 0xbfb8aa3b, v44\n"
    "v_mul_f32 v77, 0xbfb8aa3b, v45\n"
    "v_mul_f32 v78, 0xbfb8aa3b, v46\n"
    "v_mul_f32 v79, 0xbfb8aa3b, v47\n"
    "v_exp_f32 v64, v64\n"
    "v_exp_f32 v65, v65\n"
    "v_exp_f32 v66, v66\n"
    "v_exp_f32 v67, v67\n"
    "v_exp_f32 v68, v68\n"
    "v_exp_f32 v69, v69\n"
    "v_exp_f32 v70, v70\n"
    "v_exp_f32 v71, v71\n"
    "v_exp_f32 v72, v72\n"
    "v_exp_f32 v73, v73\n"
    "v_exp_f32 v74, v74\n"
    "v_exp_f32 v75, v75\n"
    "v_exp_f32 v76, v76\n"
    "v_exp_f32 v77, v77\n"
    "v_exp_f32 v78, v78\n"
    "v_exp_f32 v79, v79\n"
    "v_add_f32 v64, 1.0, v64\n"
    "v_add_f32 v65, 1.0, v65\n"
    "v_add_f32 v66, 1.0, v66\n"
    "v_add_f32 v67, 1.0, v67\n"
    "v_add_f32 v68, 1.0, v68\n"
    "v_add_f32 v69, 1.0, v69\n"
    "v_add_f32 v70, 1.0, v70\n"
    "v_add_f32 v71, 1.0, v71\n"
    "v_add_f32 v72, 1.0, v72\n"
    "v_add_f32 v73, 1.0, v73\n"
    "v_add_f32 v74, 1.0, v74\n"
    "v_add_f32 v75, 1.0, v75\n"
    "v_add_f32 v76, 1.0, v76\n"
    "v_add_f32 v77, 1.0, v77\n"
    "v_add_f32 v78, 1.0, v78\n"
    "v_add_f32 v79, 1.0, v79\n"
    "v_rcp_f32 v64, v64\n"
    "v_rcp_f32 v65, v65\n"
    "v_rcp_f32 v66, v66\n"
    "v_rcp_f32 v67, v67\n"
    "v_rcp_f32 v68, v68\n"
    "v_rcp_f32 v69, v69\n"
    "v_rcp_f32 v70, v70\n"
    "v_rcp_f32 v71, v71\n"
    "v_rcp_f32 v72, v72\n"
    "v_rcp_f32 v73, v73\n"
    "v_rcp_f32 v74, v74\n"
    "v_rcp_f32 v75, v75\n"
    "v_rcp_f32 v76, v76\n"
    "v_rcp_f32 v77, v77\n"
    "v_rcp_f32 v78, v78\n"
    "v_rcp_f32 v79, v79\n"
    "v_mul_f32 v32, v32, v64\n"
    "v_mul_f32 v33, v33, v65\n"
    "v_mul_f32 v34, v34, v66\n"
    "v_mul_f32 v35, v35, v67\n"
    "v_mul_f32 v36, v36, v68\n"
    "v_mul_f32 v37, v37, v69\n"
    "v_mul_f32 v38, v38, v70\n"
    "v_mul_f32 v39, v39, v71\n"
    "v_mul_f32 v40, v40, v72\n"
    "v_mul_f32 v41, v41, v73\n"
    "v_mul_f32 v42, v42, v74\n"
    "v_mul_f32 v43, v43, v75\n"
    "v_mul_f32 v44, v44, v76\n"
    "v_mul_f32 v45, v45, v77\n"
    "v_mul_f32 v46, v46, v78\n"
    "v_mul_f32 v47, v47, v79\n"
    "v_mul_f32 v80, 0xbfb8aa3b, v48\n"
    "v_mul_f32 v81, 0xbfb8aa3b, v49\n"
    "v_mul_f32 v82, 0xbfb8aa3b, v50\n"
    "v_mul_f32 v83, 0xbfb8aa3b, v51\n"
    "v_mul_f32 v84, 0xbfb8aa3b, v52\n"
    "v_mul_f32 v85, 0xbfb8aa3b, v53\n"
    "v_mul_f32 v86, 0xbfb8aa3b, v54\n"
    "v_mul_f32 v87, 0xbfb8aa3b, v55\n"
    "v_mul_f32 v88, 0xbfb8aa3b, v56\n"
    "v_mul_f32 v89, 0xbfb8aa3b, v57\n"
    "v_mul_f32 v90, 0xbfb8aa3b, v58\n"
    "v_mul_f32 v91, 0xbfb8aa3b, v59\n"
    "v_mul_f32 v92, 0xbfb8aa3b, v60\n"
    "v_mul_f32 v93, 0xbfb8aa3b, v61\n"
    "v_mul_f32 v94, 0xbfb8aa3b, v62\n"
    "v_mul_f32 v95, 0xbfb8aa3b, v63\n"
    "v_exp_f32 v80, v80\n"
    "v_exp_f32 v81, v81\n"
    "v_exp_f32 v82, v82\n"
    "v_exp_f32 v83, v83\n"
    "v_exp_f32 v84, v84\n"
    "v_exp_f32 v85, v85\n"
    "v_exp_f32 v86, v86\n"
    "v_exp_f32 v87, v87\n"
    "v_exp_f32 v88, v88\n"
    "v_exp_f32 v89, v89\n"
    "v_exp_f32 v90, v90\n"
    "v_exp_f32 v91, v91\n"
    "v_exp_f32 v92, v92\n"
    "v_exp_f32 v93, v93\n"
    "v_exp_f32 v94, v94\n"
    "v_exp_f32 v95, v95\n"
    "v_add_f32 v80, 1.0, v80\n"
    "v_add_f32 v81, 1.0, v81\n"
    "v_add_f32 v82, 1.0, v82\n"
    "v_add_f32 v83, 1.0, v83\n"
    "v_add_f32 v84, 1.0, v84\n"
    "v_add_f32 v85, 1.0, v85\n"
    "v_add_f32 v86, 1.0, v86\n"
    "v_add_f32 v87, 1.0, v87\n"
    "v_add_f32 v88, 1.0, v88\n"
    "v_add_f32 v89, 1.0, v89\n"
    "v_add_f32 v90, 1.0, v90\n"
    "v_add_f32 v91, 1.0, v91\n"
    "v_add_f32 v92, 1.0, v92\n"
    "v_add_f32 v93, 1.0, v93\n"
    "v_add_f32 v94, 1.0, v94\n"
    "v_add_f32 v95, 1.0, v95\n"
    "v_rcp_f32 v80, v80\n"
    "v_rcp_f32 v81, v81\n"
    "v_rcp_f32 v82, v82\n"
    "v_rcp_f32 v83, v83\n"
    "v_rcp_f32 v84, v84\n"
    "v_rcp_f32 v85, v85\n"
    "v_rcp_f32 v86, v86\n"
    "v_rcp_f32 v87, v87\n"
    "v_rcp_f32 v88, v88\n"
    "v_rcp_f32 v89, v89\n"
    "v_rcp_f32 v90, v90\n"
    "v_rcp_f32 v91, v91\n"
    "v_rcp_f32 v92, v92\n"
    "v_rcp_f32 v93, v93\n"
    "v_rcp_f32 v94, v94\n"
    "v_rcp_f32 v95, v95\n"
    "v_mul_f32 v48, v48, v80\n"
    "v_mul_f32 v49, v49, v81\n"
    "v_mul_f32 v50, v50, v82\n"
    "v_mul_f32 v51, v51, v83\n"
    "v_mul_f32 v52, v52, v84\n"
    "v_mul_f32 v53, v53, v85\n"
    "v_mul_f32 v54, v54, v86\n"
    "v_mul_f32 v55, v55, v87\n"
    "v_mul_f32 v56, v56, v88\n"
    "v_mul_f32 v57, v57, v89\n"
    "v_mul_f32 v58, v58, v90\n"
    "v_mul_f32 v59, v59, v91\n"
    "v_mul_f32 v60, v60, v92\n"
    "v_mul_f32 v61, v61, v93\n"
    "v_mul_f32 v62, v62, v94\n"
    "v_mul_f32 v63, v63, v95\n"
    "K2_ST:\n"
    "s_waitcnt vmcnt(11)\n"
    "v_cvt_pk_bf16_f32 v96, v32, v33\n"
    "v_cvt_pk_bf16_f32 v97, v34, v35\n"
    "v_cvt_pk_bf16_f32 v98, v36, v37\n"
    "v_cvt_pk_bf16_f32 v99, v38, v39\n"
    "s_waitcnt vmcnt(10)\n"
    "v_cvt_pk_bf16_f32 v100, v40, v41\n"
    "v_cvt_pk_bf16_f32 v101, v42, v43\n"
    "v_cvt_pk_bf16_f32 v102, v44, v45\n"
    "v_cvt_pk_bf16_f32 v103, v46, v47\n"
    "s_waitcnt vmcnt(9)\n"
    "v_cvt_pk_bf16_f32 v104, v48, v49\n"
    "v_cvt_pk_bf16_f32 v105, v50, v51\n"
    "v_cvt_pk_bf16_f32 v106, v52, v53\n"
    "v_cvt_pk_bf16_f32 v107, v54, v55\n"
    "s_waitcnt vmcnt(8)\n"
    "v_cvt_pk_bf16_f32 v108, v56, v57\n"
    "v_cvt_pk_bf16_f32 v109, v58, v59\n"
    "v_cvt_pk_bf16_f32 v110, v60, v61\n"
    "v_cvt_pk_bf16_f32 v111, v62, v63\n"
    "v_min_u32 v96, 0xfffffffe, v96\n"
    "v_min_u32 v97, 0xfffffffe, v97\n"
    "v_min_u32 v98, 0xfffffffe, v98\n"
    "v_min_u32 v99, 0xfffffffe, v99\n"
    "v_min_u32 v100, 0xfffffffe, v100\n"
    "v_min_u32 v101, 0xfffffffe, v101\n"
    "v_min_u32 v102, 0xfffffffe, v102\n"
    "v_min_u32 v103, 0xfffffffe, v103\n"
    "v_min_u32 v104, 0xfffffffe, v104\n"
    "v_min_u32 v105, 0xfffffffe, v105\n"
    "v_min_u32 v106, 0xfffffffe, v106\n"
    "v_min_u32 v107, 0xfffffffe, v107\n"
    "v_min_u32 v108, 0xfffffffe, v108\n"
    "v_min_u32 v109, 0xfffffffe, v109\n"
    "v_min_u32 v110, 0xfffffffe, v110\n"
    "v_min_u32 v111, 0xfffffffe, v111\n"
    "s_sub_u32 s58, s58, 1\n"
    "s_lshl_b32 s88, s58, 15\n"
    "buffer_store_dwordx4 v[96:99], v2, s[68:71], s88 offen offset:0 sc1\n"
    "buffer_store_dwordx4 v[100:103], v2, s[68:71], s88 offen offset:1024 sc1\n"
    "buffer_store_dwordx4 v[104:107], v2, s[68:71], s88 offen offset:2048 sc1\n"
    "buffer_store_dwordx4 v[108:111], v2, s[68:71], s88 offen offset:3072 sc1\n"
    "s_branch K2_END\n"
    "K2_FIN:\n"
    "s_mov_b32 s91, 0x40000\n"
    "s_waitcnt vmcnt(0)\n"
    "v_accvgpr_read_b32 v64, a32\n"
    "v_accvgpr_read_b32 v65, a33\n"
    "v_accvgpr_read_b32 v66, a34\n"
    "v_accvgpr_read_b32 v67, a35\n"
    "v_accvgpr_read_b32 v68, a36\n"
    "v_accvgpr_read_b32 v69, a37\n"
    "v_accvgpr_read_b32 v70, a38\n"
    "v_accvgpr_read_b32 v71, a39\n"
    "v_accvgpr_read_b32 v72, a40\n"
    "v_accvgpr_read_b32 v73, a41\n"
    "v_accvgpr_read_b32 v74, a42\n"
    "v_accvgpr_read_b32 v75, a43\n"
    "v_accvgpr_read_b32 v76, a44\n"
    "v_accvgpr_read_b32 v77, a45\n"
    "v_accvgpr_read_b32 v78, a46\n"
    "v_accvgpr_read_b32 v79, a47\n"
    "v_accvgpr_read_b32 v80, a48\n"
    "v_accvgpr_read_b32 v81, a49\n"
    "v_accvgpr_read_b32 v82, a50\n"
    "v_accvgpr_read_b32 v83, a51\n"
    "v_accvgpr_read_b32 v84, a52\n"
    "v_accvgpr_read_b32 v85, a53\n"
    "v_accvgpr_read_b32 v86, a54\n"
    "v_accvgpr_read_b32 v87, a55\n"
    "v_accvgpr_read_b32 v88, a56\n"
    "v_accvgpr_read_b32 v89, a57\n"
    "v_accvgpr_read_b32 v90, a58\n"
    "v_accvgpr_read_b32 v91, a59\n"
    "v_accvgpr_read_b32 v92, a60\n"
    "v_accvgpr_read_b32 v93, a61\n"
    "v_accvgpr_read_b32 v94, a62\n"
    "v_accvgpr_read_b32 v95, a63\n"
    "s_branch K2_CHK\n"
    "K2_POLL:\n"
    "buffer_load_dwordx4 v[80:83], v2, s[68:71], s88 offen offset:0 sc1\n"
    "buffer_load_dwordx4 v[84:87], v2, s[68:71], s88 offen offset:1024 sc1\n"
    "buffer_load_dwordx4 v[88:91], v2, s[68:71], s88 offen offset:2048 sc1\n"
    "buffer_load_dwordx4 v[92:95], v2, s[68:71], s88 offen offset:3072 sc1\n"
    "buffer_load_dwordx4 v[64:67], v2, s[68:71], 0 offen offset:0 sc1\n"
    "buffer_load_dwordx4 v[68:71], v2, s[68:71], 0 offen offset:1024 sc1\n"
    "buffer_load_dwordx4 v[72:75], v2, s[68:71], 0 offen offset:2048 sc1\n"
    "buffer_load_dwordx4 v[76:79], v2, s[68:71], 0 offen offset:3072 sc1\n"
    "K2_CHK:\n"
    "s_sub_u32 s91, s91, 1\n"
    "s_waitcnt vmcnt(4)\n"
    "v_max3_u32 v8, v80, v81, v82\n"
    "v_max3_u32 v8, v8, v83, v84\n"
    "v_max3_u32 v8, v8, v85, v86\n"
    "v_max3_u32 v8, v8, v87, v88\n"
    "v_max3_u32 v8, v8, v89, v90\n"
    "v_max3_u32 v8, v8, v91, v92\n"
    "v_max3_u32 v8, v8, v93, v94\n"
    "v_max_u32 v8, v8, v95\n"
    "s_waitcnt vmcnt(0)\n"
    "v_max3_u32 v9, v64, v65, v66\n"
    "v_max3_u32 v9, v9, v67, v68\n"
    "v_max3_u32 v9, v9, v69, v70\n"
    "v_max3_u32 v9, v9, v71, v72\n"
    "v_max3_u32 v9, v9, v73, v74\n"
    "v_max3_u32 v9, v9, v75, v76\n"
    "v_max3_u32 v9, v9, v77, v78\n"
    "v_max3_u32 v8, v8, v9, v79\n"
    "v_cmp_eq_u32 vcc, -1, v8\n"
    "s_cmp_eq_u32 s91, 0\n"
    "s_cbranch_scc1 K2_GO\n"
    "s_cbranch_vccnz K2_POLL\n"
    "K2_GO:\n"
    "v_lshlrev_b32 v112, 16, v64\n"
    "v_and_b32 v113, 0xffff0000, v64\n"
    "v_lshlrev_b32 v114, 16, v65\n"
    "v_and_b32 v115, 0xffff0000, v65\n"
    "v_lshlrev_b32 v116, 16, v66\n"
    "v_and_b32 v117, 0xffff0000, v66\n"
    "v_lshlrev_b32 v118, 16, v67\n"
    "v_and_b32 v119, 0xffff0000, v67\n"
    "v_lshlrev_b32 v120, 16, v80\n"
    "v_and_b32 v121, 0xffff0000, v80\n"
    "v_lshlrev_b32 v122, 16, v81\n"
    "v_and_b32 v123, 0xffff0000, v81\n"
    "v_lshlrev_b32 v124, 16, v82\n"
    "v_and_b32 v125, 0xffff0000, v82\n"
    "v_lshlrev_b32 v126, 16, v83\n"
    "v_and_b32 v127, 0xffff0000, v83\n"
    "v_add_f32 v32, v32, v112\n"
    "v_add_f32 v33, v33, v113\n"
    "v_add_f32 v34, v34, v114\n"
    "v_add_f32 v35, v35, v115\n"
    "v_add_f32 v36, v36, v116\n"
    "v_add_f32 v37, v37, v117\n"
    "v_add_f32 v38, v38, v118\n"
    "v_add_f32 v39, v39, v119\n"
    "v_mul_f32 v32, v32, v120\n"
    "v_mul_f32 v33, v33, v121\n"
    "v_mul_f32 v34, v34, v122\n"
    "v_mul_f32 v35, v35, v123\n"
    "v_mul_f32 v36, v36, v124\n"
    "v_mul_f32 v37, v37, v125\n"
    "v_mul_f32 v38, v38, v126\n"
    "v_mul_f32 v39, v39, v127\n"
    "v_permlane16_swap_b32 v32, v36\n"
    "v_permlane16_swap_b32 v33, v37\n"
    "v_permlane16_swap_b32 v34, v38\n"
    "v_permlane16_swap_b32 v35, v39\n"
    "v_lshlrev_b32 v112, 16, v96\n"
    "v_and_b32 v113, 0xffff0000, v96\n"
    "v_lshlrev_b32 v114, 16, v97\n"
    "v_and_b32 v115, 0xffff0000, v97\n"
    "v_lshlrev_b32 v116, 16, v98\n"
    "v_and_b32 v117, 0xffff0000, v98\n"
    "v_lshlrev_b32 v118, 16, v99\n"
    "v_and_b32 v119, 0xffff0000, v99\n"
    "v_add_f32 v32, v32, v112\n"
    "v_add_f32 v33, v33, v113\n"
    "v_add_f32 v34, v34, v114\n"
    "v_add_f32 v35, v35, v115\n"
    "v_add_f32 v36, v36, v116\n"
    "v_add_f32 v37, v37, v117\n"
    "v_add_f32 v38, v38, v118\n"
    "v_add_f32 v39, v39, v119\n"
    "v_cvt_pk_bf16_f32 v32, v32, v33\n"
    "v_cvt_pk_bf16_f32 v33, v34, v35\n"
    "v_cvt_pk_bf16_f32 v34, v36, v37\n"
    "v_cvt_pk_bf16_f32 v35, v38, v39\n"
    "buffer_store_dwordx4 v[32:35], v4, s[84:87], 0 offen offset:0 sc1\n"
    "v_lshlrev_b32 v112, 16, v68\n"
    "v_and_b32 v113, 0xffff0000, v68\n"
    "v_lshlrev_b32 v114, 16, v69\n"
    "v_and_b32 v115, 0xffff0000, v69\n"
    "v_lshlrev_b32 v116, 16, v70\n"
    "v_and_b32 v117, 0xffff0000, v70\n"
    "v_lshlrev_b32 v118, 16, v71\n"
    "v_and_b32 v119, 0xffff0000, v71\n"
    "v_lshlrev_b32 v120, 16, v84\n"
    "v_and_b32 v121, 0xffff0000, v84\n"
    "v_lshlrev_b32 v122, 16, v85\n"
    "v_and_b32 v123, 0xffff0000, v85\n"
    "v_lshlrev_b32 v124, 16, v86\n"
    "v_and_b32 v125, 0xffff0000, v86\n"
    "v_lshlrev_b32 v126, 16, v87\n"
    "v_and_b32 v127, 0xffff0000, v87\n"
    "v_add_f32 v40, v40, v112\n"
    "v_add_f32 v41, v41, v113\n"
    "v_add_f32 v42, v42, v114\n"
    "v_add_f32 v43, v43, v115\n"
    "v_add_f32 v44, v44, v116\n"
    "v_add_f32 v45, v45, v117\n"
    "v_add_f32 v46, v46, v118\n"
    "v_add_f32 v47, v47, v119\n"
    "v_mul_f32 v40, v40, v120\n"
    "v_mul_f32 v41, v41, v121\n"
    "v_mul_f32 v42, v42, v122\n"
    "v_mul_f32 v43, v43, v123\n"
    "v_mul_f32 v44, v44, v124\n"
    "v_mul_f32 v45, v45, v125\n"
    "v_mul_f32 v46, v46, v126\n"
    "v_mul_f32 v47, v47, v127\n"
    "v_permlane16_swap_b32 v40, v44\n"
    "v_permlane16_swap_b32 v41, v45\n"
    "v_permlane16_swap_b32 v42, v46\n"
    "v_permlane16_swap_b32 v43, v47\n"
    "v_lshlrev_b32 v112, 16, v100\n"
    "v_and_b32 v113, 0xffff0000, v100\n"
    "v_lshlrev_b32 v114, 16, v101\n"
    "v_and_b32 v115, 0xffff0000, v101\n"
    "v_lshlrev_b32 v116, 16, v102\n"
    "v_and_b32 v117, 0xffff0000, v102\n"
    "v_lshlrev_b32 v118, 16, v103\n"
    "v_and_b32 v119, 0xffff0000, v103\n"
    "v_add_f32 v40, v40, v112\n"
    "v_add_f32 v41, v41, v113\n"
    "v_add_f32 v42, v42, v114\n"
    "v_add_f32 v43, v43, v115\n"
    "v_add_f32 v44, v44, v116\n"
    "v_add_f32 v45, v45, v117\n"
    "v_add_f32 v46, v46, v118\n"
    "v_add_f32 v47, v47, v119\n"
    "v_cvt_pk_bf16_f32 v40, v40, v41\n"
    "v_cvt_pk_bf16_f32 v41, v42, v43\n"
    "v_cvt_pk_bf16_f32 v42, v44, v45\n"
    "v_cvt_pk_bf16_f32 v43, v46, v47\n"
    "buffer_store_dwordx4 v[40:43], v4, s[84:87], 0 offen offset:64 sc1\n"
    "v_lshlrev_b32 v112, 16, v72\n"
    "v_and_b32 v113, 0xffff0000, v72\n"
    "v_lshlrev_b32 v114, 16, v73\n"
    "v_and_b32 v115, 0xffff0000, v73\n"
    "v_lshlrev_b32 v116, 16, v74\n"
    "v_and_b32 v117, 0xffff0000, v74\n"
    "v_lshlrev_b32 v118, 16, v75\n"
    "v_and_b32 v119, 0xffff0000, v75\n"
    "v_lshlrev_b32 v120, 16, v88\n"
    "v_and_b32 v121, 0xffff0000, v88\n"
    "v_lshlrev_b32 v122, 16, v89\n"
    "v_and_b32 v123, 0xffff0000, v89\n"
    "v_lshlrev_b32 v124, 16, v90\n"
    "v_and_b32 v125, 0xffff0000, v90\n"
    "v_lshlrev_b32 v126, 16, v91\n"
    "v_and_b32 v127, 0xffff0000, v91\n"
    "v_add_f32 v48, v48, v112\n"
    "v_add_f32 v49, v49, v113\n"
    "v_add_f32 v50, v50, v114\n"
    "v_add_f32 v51, v51, v115\n"
    "v_add_f32 v52, v52, v116\n"
    "v_add_f32 v53, v53, v117\n"
    "v_add_f32 v54, v54, v118\n"
    "v_add_f32 v55, v55, v119\n"
    "v_mul_f32 v48, v48, v120\n"
    "v_mul_f32 v49, v49, v121\n"
    "v_mul_f32 v50, v50, v122\n"
    "v_mul_f32 v51, v51, v123\n"
    "v_mul_f32 v52, v52, v124\n"
    "v_mul_f32 v53, v53, v125\n"
    "v_mul_f32 v54, v54, v126\n"
    "v_mul_f32 v55, v55, v127\n"
    "v_permlane16_swap_b32 v48, v52\n"
    "v_permlane16_swap_b32 v49, v53\n"
    "v_permlane16_swap_b32 v50, v54\n"
    "v_permlane16_swap_b32 v51, v55\n"
    "v_lshlrev_b32 v112, 16, v104\n"
    "v_and_b32 v113, 0xffff0000, v104\n"
    "v_lshlrev_b32 v114, 16, v105\n"
    "v_and_b32 v115, 0xffff0000, v105\n"
    "v_lshlrev_b32 v116, 16, v106\n"
    "v_and_b32 v117, 0xffff0000, v106\n"
    "v_lshlrev_b32 v118, 16, v107\n"
    "v_and_b32 v119, 0xffff0000, v107\n"
    "v_add_f32 v48, v48, v112\n"
    "v_add_f32 v49, v49, v113\n"
    "v_add_f32 v50, v50, v114\n"
    "v_add_f32 v51, v51, v115\n"
    "v_add_f32 v52, v52, v116\n"
    "v_add_f32 v53, v53, v117\n"
    "v_add_f32 v54, v54, v118\n"
    "v_add_f32 v55, v55, v119\n"
    "v_cvt_pk_bf16_f32 v48, v48, v49\n"
    "v_cvt_pk_bf16_f32 v49, v50, v51\n"
    "v_cvt_pk_bf16_f32 v50, v52, v53\n"
    "v_cvt_pk_bf16_f32 v51, v54, v55\n"
    "buffer_store_dwordx4 v[48:51], v4, s[84:87], 0 offen offset:128 sc1\n"
    "v_lshlrev_b32 v112, 16, v76\n"
    "v_and_b32 v113, 0xffff0000, v76\n"
    "v_lshlrev_b32 v114, 16, v77\n"
    "v_and_b32 v115, 0xffff0000, v77\n"
    "v_lshlrev_b32 v116, 16, v78\n"
    "v_and_b32 v117, 0xffff0000, v78\n"
    "v_lshlrev_b32 v118, 16, v79\n"
    "v_and_b32 v119, 0xffff0000, v79\n"
    "v_lshlrev_b32 v120, 16, v92\n"
    "v_and_b32 v121, 0xffff0000, v92\n"
    "v_lshlrev_b32 v122, 16, v93\n"
    "v_and_b32 v123, 0xffff0000, v93\n"
    "v_lshlrev_b32 v124, 16, v94\n"
    "v_and_b32 v125, 0xffff0000, v94\n"
    "v_lshlrev_b32 v126, 16, v95\n"
    "v_and_b32 v127, 0xffff0000, v95\n"
    "v_add_f32 v56, v56, v112\n"
    "v_add_f32 v57, v57, v113\n"
    "v_add_f32 v58, v58, v114\n"
    "v_add_f32 v59, v59, v115\n"
    "v_add_f32 v60, v60, v116\n"
    "v_add_f32 v61, v61, v117\n"
    "v_add_f32 v62, v62, v118\n"
    "v_add_f32 v63, v63, v119\n"
    "v_mul_f32 v56, v56, v120\n"
    "v_mul_f32 v57, v57, v121\n"
    "v_mul_f32 v58, v58, v122\n"
    "v_mul_f32 v59, v59, v123\n"
    "v_mul_f32 v60, v60, v124\n"
    "v_mul_f32 v61, v61, v125\n"
    "v_mul_f32 v62, v62, v126\n"
    "v_mul_f32 v63, v63, v127\n"
    "v_permlane16_swap_b32 v56, v60\n"
    "v_permlane16_swap_b32 v57, v61\n"
    "v_permlane16_swap_b32 v58, v62\n"
    "v_permlane16_swap_b32 v59, v63\n"
    "v_lshlrev_b32 v112, 16, v108\n"
    "v_and_b32 v113, 0xffff0000, v108\n"
    "v_lshlrev_b32 v114, 16, v109\n"
    "v_and_b32 v115, 0xffff0000, v109\n"
    "v_lshlrev_b32 v116, 16, v110\n"
    "v_and_b32 v117, 0xffff0000, v110\n"
    "v_lshlrev_b32 v118, 16, v111\n"
    "v_and_b32 v119, 0xffff0000, v111\n"
    "v_add_f32 v56, v56, v112\n"
    "v_add_f32 v57, v57, v113\n"
    "v_add_f32 v58, v58, v114\n"
    "v_add_f32 v59, v59, v115\n"
    "v_add_f32 v60, v60, v116\n"
    "v_add_f32 v61, v61, v117\n"
    "v_add_f32 v62, v62, v118\n"
    "v_add_f32 v63, v63, v119\n"
    "v_cvt_pk_bf16_f32 v56, v56, v57\n"
    "v_cvt_pk_bf16_f32 v57, v58, v59\n"
    "v_cvt_pk_bf16_f32 v58, v60, v61\n"
    "v_cvt_pk_bf16_f32 v59, v62, v63\n"
    "buffer_store_dwordx4 v[56:59], v4, s[84:87], 0 offen offset:192 sc1\n"
    "K2_END:\n"
    "s_waitcnt vmcnt(0)\n"
    :: "s"(ws+WS_ACT),"s"(wo),"s"(wd),"s"(x),"s"(out),"s"(ws+WS_PART),"s"(blockIdx.x),"v"(threadIdx.x)
    : "v1","v2","v3","v4","v5","v6","v7","v8","v9","v10","v11","v12","v13","v14","v15","v16","v17","v18","v19","v20","v21","v22","v23","v24","v25","v26","v27","v28","v29","v30","v31","v32","v33","v34","v35","v36","v37","v38","v39","v40","v41","v42","v43","v44","v45","v46","v47","v48","v49","v50","v51","v52","v53","v54","v55","v56","v57","v58","v59","v60","v61","v62","v63","v64","v65","v66","v67","v68","v69","v70","v71","v72","v73","v74","v75","v76","v77","v78","v79","v80","v81","v82","v83","v84","v85","v86","v87","v88","v89","v90","v91","v92","v93","v94","v95","v96","v97","v98","v99","v100","v101","v102","v103","v104","v105","v106","v107","v108","v109","v110","v111","v112","v113","v114","v115","v116","v117","v118","v119","v120","v121","v122","v123","v124","v125","v126","v127","a0","a1","a2","a3","a4","a5","a6","a7","a8","a9","a10","a11","a12","a13","a14","a15","a16","a17","a18","a19","a20","a21","a22","a23","a24","a25","a26","a27","a28","a29","a30","a31","a32","a33","a34","a35","a36","a37","a38","a39","a40","a41","a42","a43","a44","a45","a46","a47","a48","a49","a50","a51","a52","a53","a54","a55","a56","a57","a58","a59","a60","a61","a62","a63","a64","a65","a66","a67","a68","a69","a70","a71","a72","a73","a74","a75","a76","a77","a78","a79","a80","a81","a82","a83","a84","a85","a86","a87","a88","a89","a90","a91","a92","a93","a94","a95","a96","a97","a98","a99","a100","a101","a102","a103","a104","a105","a106","a107","a108","a109","a110","a111","a112","a113","a114","a115","a116","a117","a118","a119","a120","a121","a122","a123","a124","a125","a126","a127","s20","s21","s22","s23","s24","s25","s26","s27","s28","s29","s30","s31","s33","s34","s35","s36","s37","s38","s39","s40","s41","s42","s43","s44","s45","s46","s47","s48","s49","s50","s51","s52","s53","s54","s55","s56","s57","s58","s59","s60","s61","s62","s63","s64","s65","s66","s67","s68","s69","s70","s71","s72","s73","s74","s75","s76","s77","s78","s79","s80","s81","s82","s83","s84","s85","s86","s87","s88","s89","s90","s91","s92","s93","s94","s95","s96","s97","s98","s99","vcc","scc","memory");
}

__device__ __forceinline__ void slow_attn(char* ws,const u16* kc,const u16* vc,const u16* wq,int blk,int wv){
  const int lane=__builtin_amdgcn_mbcnt_hi(-1,__builtin_amdgcn_mbcnt_lo(-1,0));
  const int head=4*(blk&7)+((blk>>3)&3), rb=blk>>5; const int row=rb*256+wv*64+lane; const int R=row>>4, r=row&15;
  const u16* xf=(const u16*)(ws+WS_XNF)+(size_t)R*20*512;
  const u16* K=kc+((size_t)head*2048+1536)*128; const u16* V=vc+((size_t)head*2048+1536)*128;
  u16* of=(u16*)(ws+WS_ACT)+((size_t)R*184+head*4)*512;
  float q[128];
  #pragma unroll
  for(int d=0;d<128;d++) q[d]=0.f;
  #pragma unroll 1
  for(int k=0;k<640;k++){ const int kk=k&31; const float xv=b2f(xf[(k>>5)*512+(r+16*((kk>>2)&3))*8+(kk>>4)*4+(kk&3)]);
    const u32* w=(const u32*)(wq+(size_t)k*4096+head*128);
    #pragma unroll
    for(int d=0;d<64;d++){ const u32 u=w[d]; q[2*d]+=xv*__uint_as_float(u<<16); q[2*d+1]+=xv*__uint_as_float(u&0xffff0000u); } }
  float mx=-INFINITY, sum=0.f;
  #pragma unroll 1
  for(int pass=0;pass<2;pass++){
    #pragma unroll 1
    for(int key=0;key<512;key++){ const u32* kp=(const u32*)(K+key*128); float s=0.f;
      #pragma unroll
      for(int d=0;d<64;d++){ const u32 u=kp[d]; s+=q[2*d]*__uint_as_float(u<<16)+q[2*d+1]*__uint_as_float(u&0xffff0000u); }
      s*=0.08838834764831845f;
      if(pass==0) mx=fmaxf(mx,s); else sum+=__expf(s-mx); } }
  const float inv=1.f/sum;
  #pragma unroll 1
  for(int c=0;c<4;c++){ float o[32];
    #pragma unroll
    for(int d=0;d<32;d++) o[d]=0.f;
    #pragma unroll 1
    for(int key=0;key<512;key++){ const u32* kp=(const u32*)(K+key*128); float s=0.f;
      #pragma unroll
      for(int d=0;d<64;d++){ const u32 u=kp[d]; s+=q[2*d]*__uint_as_float(u<<16)+q[2*d+1]*__uint_as_float(u&0xffff0000u); }
      const float p=__expf(s*0.08838834764831845f-mx)*inv; const u32* vp=(const u32*)(V+key*128+c*32);
      #pragma unroll
      for(int d=0;d<16;d++){ const u32 u=vp[d]; o[2*d]+=p*__uint_as_float(u<<16); o[2*d+1]+=p*__uint_as_float(u&0xffff0000u); } }
    #pragma unroll
    for(int d=0;d<32;d++) of[c*512+(r+16*((d>>2)&3))*8+(d>>4)*4+(d&3)]=f2b(o[d]); }
}

#define K1_LDS 143360
__global__ __launch_bounds__(256,1) void k1_main(char* ws,const u16* kc,const u16* vc,const u16* wq,const u16* wg,const u16* wu,const u16* wo,const u16* wd){
  int flag; const int wv=__builtin_amdgcn_readfirstlane(threadIdx.x>>6);
  asm volatile(
    "s_mov_b64 s[60:61], %1\n"
    "s_mov_b64 s[62:63], %2\n"
    "s_mov_b64 s[98:99], %3\n"
    "s_mov_b64 s[34:35], %5\n"
    "s_mov_b64 s[56:57], %6\n"
    "s_mov_b32 s72, %7\n"
    "v_mov_b32 v1, %8\n"
    "v_lshrrev_b32 v20, 6, v1\n"
    "s_nop 0\n"
    "v_readfirstlane_b32 s73, v20\n"
    "v_and_b32 v1, 63, v1\n"
    "s_mov_b64 s[96:97], %4\n"
    "s_and_b32 s74, s72, 7\n"
    "s_lshr_b32 s75, s72, 3\n"
    "s_lshl_b32 s74, s74, 2\n"
    "s_and_b32 s76, s75, 3\n"
    "s_add_u32 s74, s74, s76\n"
    "s_lshr_b32 s75, s75, 2\n"
    "s_lshl_b32 s45, s73, 10\n"
    "s_mul_i32 s59, s73, 1056\n"
    "s_add_u32 s64, s59, 0\n"
    "s_add_u32 s65, s59, 8448\n"
    "s_add_u32 s66, s59, 16896\n"
    "s_add_u32 s67, s59, 25344\n"
    "s_add_u32 s68, s59, 4224\n"
    "s_add_u32 s69, s59, 12672\n"
    "s_add_u32 s70, s59, 21120\n"
    "s_add_u32 s71, s59, 29568\n"
    "s_mov_b32 s51, 33792\n"
    "s_mov_b32 s43, 0\n"
    "s_mov_b32 s44, s51\n"
    "s_mov_b32 s40, 0\n"
    "s_lshl_b32 s76, s74, 8\n"
    "s_add_u32 s20, s96, s76\n"
    "s_addc_u32 s21, s97, 0\n"
    "s_mov_b32 s22, 0x7fffffff\n"
    "s_mov_b32 s23, 0x20000\n"
    "s_add_u32 s24, s20, 128\n"
    "s_addc_u32 s25, s21, 0\n"
    "s_mov_b32 s26, 0x7fffffff\n"
    "s_mov_b32 s27, 0x20000\n"
    "s_mov_b32 s41, 1048576\n"
    "s_mov_b32 s33, 0\n"
    "v_lshrrev_b32 v20, 4, v1\n"
    "v_lshlrev_b32 v23, 3, v20\n"
    "v_lshrrev_b32 v20, 3, v1\n"
    "v_and_b32 v20, 1, v20\n"
    "s_lshl_b32 s78, s73, 1\n"
    "v_add3_u32 v23, v23, v20, s78\n"
    "v_and_b32 v24, 7, v1\n"
    "v_lshlrev_b32 v24, 4, v24\n"
    "v_lshlrev_b32 v2, 4, v1\n"
    "v_mul_u32_u24 v26, 8192, v23\n"
    "v_add_u32 v10, v26, v24\n"
    "v_add_u32 v11, 262144, v10\n"
    "v_add_u32 v12, 524288, v10\n"
    "v_add_u32 v13, 786432, v10\n"
    "v_mov_b32 v14, v10\n"
    "v_mov_b32 v15, v11\n"
    "v_mov_b32 v16, v12\n"
    "v_mov_b32 v17, v13\n"
    "s_add_u32 m0, s40, s64\n"
    "s_nop 0\n"
    "buffer_load_dwordx4 v10, s[20:23], s33 offen lds\n"
    "s_add_u32 m0, s40, s65\n"
    "s_nop 0\n"
    "buffer_load_dwordx4 v11, s[20:23], s33 offen lds\n"
    "s_add_u32 m0, s40, s66\n"
    "s_nop 0\n"
    "buffer_load_dwordx4 v12, s[20:23], s33 offen lds\n"
    "s_add_u32 m0, s40, s67\n"
    "s_nop 0\n"
    "buffer_load_dwordx4 v13, s[20:23], s33 offen lds\n"
    "s_add_u32 m0, s40, s68\n"
    "s_nop 0\n"
    "buffer_load_dwordx4 v14, s[24:27], s33 offen lds\n"
    "s_add_u32 m0, s40, s69\n"
    "s_nop 0\n"
    "buffer_load_dwordx4 v15, s[24:27], s33 offen lds\n"
    "s_add_u32 m0, s40, s70\n"
    "s_nop 0\n"
    "buffer_load_dwordx4 v16, s[24:27], s33 offen lds\n"
    "s_add_u32 m0, s40, s71\n"
    "s_nop 0\n"
    "buffer_load_dwordx4 v17, s[24:27], s33 offen lds\n"
    "s_add_u32 s33, s33, s41\n"
    "s_add_u32 s40, s40, s51\n"
    "s_mov_b32 s46, 20480\n"
    "s_mov_b32 s47, 40960\n"
    "s_mov_b32 s48, 61440\n"
    "s_lshl_b32 s77, s75, 4\n"
    "s_lshl_b32 s78, s73, 2\n"
    "s_add_u32 s77, s77, s78\n"
    "s_mul_i32 s78, s77, 20480\n"
    "s_add_u32 s28, s60, s78\n"
    "s_addc_u32 s29, s61, 0\n"
    "s_mov_b32 s30, 0x7fffffff\n"
    "s_mov_b32 s31, 0x20000\n"
    "v_mov_b32 v4, v2\n"
    "buffer_load_dwordx4 a[144:147], v4, s[28:31], 0 offen offset:0\n"
    "buffer_load_dwordx4 a[148:151], v4, s[28:31], s46 offen offset:0\n"
    "buffer_load_dwordx4 a[152:155], v4, s[28:31], s47 offen offset:0\n"
    "buffer_load_dwordx4 a[156:159], v4, s[28:31], s48 offen offset:0\n"
    "v_mov_b32 v240, 0\n"
    "v_mov_b32 v241, 0\n"
    "v_mov_b32 v242, 0\n"
    "v_mov_b32 v243, 0\n"
    "s_nop 1\n"
    "v_mfma_f32_32x32x16_bf16 v[32:47], v[240:243], v[240:243], 0\n"
    "buffer_load_dwordx4 a[160:163], v4, s[28:31], 0 offen offset:1024\n"
    "v_mfma_f32_32x32x16_bf16 v[48:63], v[240:243], v[240:243], 0\n"
    "buffer_load_dwordx4 a[164:167], v4, s[28:31], s46 offen offset:1024\n"
    "v_mfma_f32_32x32x16_bf16 v[64:79], v[240:243], v[240:243], 0\n"
    "buffer_load_dwordx4 a[168:171], v4, s[28:31], s47 offen offset:1024\n"
    "v_mfma_f32_32x32x16_bf16 v[80:95], v[240:243], v[240:243], 0\n"
    "buffer_load_dwordx4 a[172:175], v4, s[28:31], s48 offen offset:1024\n"
    "v_mfma_f32_32x32x16_bf16 v[96:111], v[240:243], v[240:243], 0\n"
    "buffer_load_dwordx4 a[176:179], v4, s[28:31], 0 offen offset:2048\n"
    "v_mfma_f32_32x32x16_bf16 v[112:127], v[240:243], v[240:243], 0\n"
    "buffer_load_dwordx4 a[180:183], v4, s[28:31], s46 offen offset:2048\n"
    "v_mfma_f32_32x32x16_bf16 v[128:143], v[240:243], v[240:243], 0\n"
    "buffer_load_dwordx4 a[184:187], v4, s[28:31], s47 offen offset:2048\n"
    "v_mfma_f32_32x32x16_bf16 v[144:159], v[240:243], v[240:243], 0\n"
    "buffer_load_dwordx4 a[188:191], v4, s[28:31], s48 offen offset:2048\n"
    "v_mfma_f32_32x32x16_bf16 a[0:15], v[240:243], v[240:243], 0\n"
    "v_mfma_f32_32x32x16_bf16 a[16:31], v[240:243], v[240:243], 0\n"
    "v_mfma_f32_32x32x16_bf16 a[32:47], v[240:243], v[240:243], 0\n"
    "v_mfma_f32_32x32x16_bf16 a[48:63], v[240:243], v[240:243], 0\n"
    "v_mfma_f32_32x32x16_bf16 a[64:79], v[240:243], v[240:243], 0\n"
    "v_mfma_f32_32x32x16_bf16 a[80:95], v[240:243], v[240:243], 0\n"
    "v_mfma_f32_32x32x16_bf16 a[96:111], v[240:243], v[240:243], 0\n"
    "v_mfma_f32_32x32x16_bf16 a[112:127], v[240:243], v[240:243], 0\n"
    "v_mfma_f32_32x32x16_bf16 a[128:143], v[240:243], v[240:243], 0\n"
    "s_add_u32 m0, s40, s64\n"
    "s_nop 0\n"
    "buffer_load_dwordx4 v10, s[20:23], s33 offen lds\n"
    "s_add_u32 m0, s40, s65\n"
    "s_nop 0\n"
    "buffer_load_dwordx4 v11, s[20:23], s33 offen lds\n"
    "s_add_u32 m0, s40, s66\n"
    "s_nop 0\n"
    "buffer_load_dwordx4 v12, s[20:23], s33 offen lds\n"
    "s_add_u32 m0, s40, s67\n"
    "s_nop 0\n"
    "buffer_load_dwordx4 v13, s[20:23], s33 offen lds\n"
    "s_add_u32 m0, s40, s68\n"
    "s_nop 0\n"
    "buffer_load_dwordx4 v14, s[24:27], s33 offen lds\n"
    "s_add_u32 m0, s40, s69\n"
    "s_nop 0\n"
    "buffer_load_dwordx4 v15, s[24:27], s33 offen lds\n"
    "s_add_u32 m0, s40, s70\n"
    "s_nop 0\n"
    "buffer_load_dwordx4 v16, s[24:27], s33 offen lds\n"
    "s_add_u32 m0, s40, s71\n"
    "s_nop 0\n"
    "buffer_load_dwordx4 v17, s[24:27], s33 offen lds\n"
    "s_add_u32 s33, s33, s41\n"
    "s_add_u32 s40, s40, s51\n"
    "s_mov_b32 s49, 4096\n"
    "v_mov_b32 v19, v2\n"
    "v_add_u32 v4, s49, v2\n"
    "v_lshrrev_b32 v22, 4, v1\n"
    "s_lshl_b32 s81, s73, 2\n"
    "v_add_u32 v27, s81, v22\n"
    "v_and_b32 v28, 15, v1\n"
    "v_xor_b32 v28, v28, v27\n"
    "v_lshlrev_b32 v28, 4, v28\n"
    "v_lshl_add_u32 v28, v27, 8, v28\n"
    "v_lshl_add_u32 v25, v23, 8, v24\n"
    "v_mul_u32_u24 v26, 3456, v23\n"
    "v_add_u32 v160, v26, v24\n"
    "v_add_u32 v164, 0, v28\n"
    "v_add_u32 v168, 0, v25\n"
    "v_add_u32 v165, 4096, v28\n"
    "v_add_u32 v169, 8192, v25\n"
    "v_add_u32 v161, 110592, v160\n"
    "v_add_u32 v166, 8192, v28\n"
    "v_add_u32 v170, 128, v25\n"
    "v_add_u32 v162, 221184, v160\n"
    "v_add_u32 v167, 12288, v28\n"
    "v_add_u32 v171, 8320, v25\n"
    "v_add_u32 v163, 331776, v160\n"
    "s_mul_i32 s82, s74, 524288\n"
    "s_add_u32 s82, s82, 393216\n"
    "s_add_u32 s52, s62, s82\n"
    "s_addc_u32 s53, s63, 0\n"
    "s_mov_b32 s54, 8192\n"
    "s_mov_b32 s55, 0x20000\n"
    "s_add_u32 m0, s45, 135168\n"
    "s_nop 0\n"
    "buffer_load_dwordx4 v164, s[52:55], 0 offen lds\n"
    "s_add_u32 m0, s45, 139264\n"
    "s_nop 0\n"
    "buffer_load_dwordx4 v165, s[52:55], 0 offen lds\n"
    "s_add_u32 s84, s52, 8192\n"
    "s_addc_u32 s85, s53, 0\n"
    "s_add_u32 s86, s98, s82\n"
    "s_addc_u32 s87, s99, 0\n"
    "s_lshl_b32 s79, s75, 2\n"
    "s_add_u32 s79, s79, s73\n"
    "s_mul_i32 s79, s79, 1024\n"
    "v_add_u32 v30, s79, v2\n"
    "global_load_dwordx4 v[244:247], v30, s[52:53] offset:0\n"
    "global_load_dwordx4 v[244:247], v30, s[86:87] offset:0\n"
    "s_lshl_b32 s90, s74, 7\n"
    "s_add_u32 s88, s34, s90\n"
    "s_addc_u32 s89, s35, 0\n"
    "s_add_u32 s92, s56, s90\n"
    "s_addc_u32 s93, s57, 0\n"
    "s_cmp_lt_u32 s74, 27\n"
    "s_cselect_b32 s91, 0x7fffffff, 0\n"
    "s_lshl_b32 s76, s72, 2\n"
    "s_add_u32 s76, s76, s73\n"
    "s_mov_b64 s[80:81], s[34:35]\n"
    "s_mov_b32 s82, 2211840\n"
    "s_mov_b32 s79, s76\n"
    "s_cmp_ge_u32 s76, 186\n"
    "s_cselect_b32 s80, s56, s80\n"
    "s_cselect_b32 s81, s57, s81\n"
    "s_cselect_b32 s78, 186, 0\n"
    "s_mov_b64 s[94:95], %9\n"
    "s_cmp_ge_u32 s76, 372\n"
    "s_cselect_b32 s80, s94, s80\n"
    "s_cselect_b32 s81, s95, s81\n"
    "s_cselect_b32 s78, 372, s78\n"
    "s_cselect_b32 s82, 5242880, s82\n"
    "s_mov_b64 s[94:95], %10\n"
    "s_cmp_ge_u32 s76, 814\n"
    "s_cselect_b32 s80, s94, s80\n"
    "s_cselect_b32 s81, s95, s81\n"
    "s_cselect_b32 s78, 814, s78\n"
    "s_cselect_b32 s82, 2211840, s82\n"
    "s_sub_u32 s79, s79, s78\n"
    "s_mul_i32 s79, s79, 12288\n"
    "v_add_u32 v29, s79, v2\n"
    "s_mov_b32 s83, 0x20000\n"
    "s_bfe_u32 s78, s72, 0x20003\n"
    "s_lshl_b32 s78, s78, 2\n"
    "s_add_u32 s78, s78, s73\n"
    "s_getpc_b64 s[96:97]\n"
    "s_mul_i32 s79, s78, 2048\n"
    "s_add_u32 s96, s96, s79\n"
    "s_addc_u32 s97, s97, 0\n"
    "s_sub_u32 s96, s96, 8600\n"
    "s_subb_u32 s97, s97, 0\n"
    "global_load_dwordx4 v[244:247], v2, s[96:97] offset:0\n"
    "global_load_dwordx4 v[244:247], v2, s[96:97] offset:1024\n"
    "s_mul_i32 s78, s77, 188416\n"
    "s_add_u32 s36, s60, s78\n"
    "s_addc_u32 s37, s61, 0\n"
    "s_add_u32 s36, s36, 4194304\n"
    "s_addc_u32 s37, s37, 0\n"
    "s_mov_b32 s38, 0x7fffffff\n"
    "s_mov_b32 s39, 0x20000\n"
    "v_lshrrev_b32 v20, 4, v1\n"
    "v_and_b32 v20, 1, v20\n"
    "v_lshrrev_b32 v21, 3, v1\n"
    "v_and_b32 v21, 1, v21\n"
    "v_lshl_add_u32 v20, v20, 1, v21\n"
    "v_mul_u32_u24 v20, 1056, v20\n"
    "v_lshrrev_b32 v21, 2, v1\n"
    "v_and_b32 v21, 1, v21\n"
    "v_lshl_add_u32 v20, v21, 7, v20\n"
    "v_lshrrev_b32 v21, 5, v1\n"
    "v_lshl_add_u32 v20, v21, 8, v20\n"
    "v_and_b32 v21, 3, v1\n"
    "v_lshl_add_u32 v18, v21, 3, v20\n"
    "v_lshl_add_u32 v3, v21, 4, v20\n"
    "v_and_b32 v20, 15, v1\n"
    "v_add_u32 v21, 0, v22\n"
    "v_xor_b32 v21, v21, v20\n"
    "v_lshlrev_b32 v21, 4, v21\n"
    "v_lshl_add_u32 v172, v20, 8, v21\n"
    "v_add_u32 v6, 33792, v172\n"
    "v_add_u32 v172, 135168, v172\n"
    "v_add_u32 v21, 4, v22\n"
    "v_xor_b32 v21, v21, v20\n"
    "v_lshlrev_b32 v21, 4, v21\n"
    "v_lshl_add_u32 v173, v20, 8, v21\n"
    "v_add_u32 v7, 33792, v173\n"
    "v_add_u32 v173, 135168, v173\n"
    "v_add_u32 v21, 8, v22\n"
    "v_xor_b32 v21, v21, v20\n"
    "v_lshlrev_b32 v21, 4, v21\n"
    "v_lshl_add_u32 v174, v20, 8, v21\n"
    "v_add_u32 v8, 33792, v174\n"
    "v_add_u32 v174, 135168, v174\n"
    "v_add_u32 v21, 12, v22\n"
    "v_xor_b32 v21, v21, v20\n"
    "v_lshlrev_b32 v21, 4, v21\n"
    "v_lshl_add_u32 v175, v20, 8, v21\n"
    "v_add_u32 v9, 33792, v175\n"
    "v_add_u32 v175, 135168, v175\n"
    "v_add_u32 v5, 33792, v18\n"
    "v_mov_b32 v20, 0x3f803f80\n"
    "v_accvgpr_write_b32 a240, v20\n"
    "v_accvgpr_write_b32 a241, v20\n"
    "v_accvgpr_write_b32 a242, v20\n"
    "v_accvgpr_write_b32 a243, v20\n"
    "s_waitcnt vmcnt(26)\n"
    "s_barrier\n"
    "ds_read_b64_tr_b16 a[208:209], v3 offset:0\n"
    "ds_read_b64_tr_b16 a[210:211], v3 offset:512\n"
    "ds_read_b64_tr_b16 a[212:213], v3 offset:8\n"
    "ds_read_b64_tr_b16 a[214:215], v3 offset:520\n"
    "ds_read_b64_tr_b16 a[216:217], v3 offset:64\n"
    "ds_read_b64_tr_b16 a[218:219], v3 offset:576\n"
    "ds_read_b64_tr_b16 a[220:221], v3 offset:72\n"
    "ds_read_b64_tr_b16 a[222:223], v3 offset:584\n"
    "ds_read_b64_tr_b16 a[224:225], v3 offset:4224\n"
    "ds_read_b64_tr_b16 a[226:227], v3 offset:4736\n"
    "ds_read_b64_tr_b16 a[228:229], v3 offset:4232\n"
    "ds_read_b64_tr_b16 a[230:231], v3 offset:4744\n"
    "s_mov_b32 s50, 5\n"
    "K1_Q:\n"
    "s_waitcnt vmcnt(16) lgkmcnt(10)\n"
    "v_mfma_f32_16x16x32_bf16 v[32:35], a[208:211], a[144:147], v[32:35]\n"
    "s_waitcnt vmcnt(12)\n"
    "ds_read_b64_tr_b16 a[232:233], v3 offset:4288\n"
    "v_mfma_f32_16x16x32_bf16 v[64:67], a[208:211], a[148:151], v[64:67]\n"
    "ds_read_b64_tr_b16 a[234:235], v3 offset:4800\n"
    "ds_read_b64_tr_b16 a[236:237], v3 offset:4296\n"
    "v_mfma_f32_16x16x32_bf16 v[96:99], a[208:211], a[152:155], v[96:99]\n"
    "ds_read_b64_tr_b16 a[238:239], v3 offset:4808\n"
    "buffer_load_dwordx4 a[192:195], v19, s[28:31], 0 offen offset:3072\n"
    "v_mfma_f32_16x16x32_bf16 v[128:131], a[208:211], a[156:159], v[128:131]\n"
    "s_waitcnt lgkmcnt(8)\n"
    "s_sub_u32 s50, s50, 1\n"
    "v_mfma_f32_16x16x32_bf16 v[36:39], a[212:215], a[144:147], v[36:39]\n"
    "v_mfma_f32_16x16x32_bf16 v[68:71], a[212:215], a[148:151], v[68:71]\n"
    "s_add_u32 m0, s40, s64\n"
    "ds_read_b64_tr_b16 a[208:209], v3 offset:8448\n"
    "v_mfma_f32_16x16x32_bf16 v[100:103], a[212:215], a[152:155], v[100:103]\n"
    "ds_read_b64_tr_b16 a[210:211], v3 offset:8960\n"
    "v_mfma_f32_16x16x32_bf16 v[132:135], a[212:215], a[156:159], v[132:135]\n"
    "s_waitcnt lgkmcnt(8)\n"
    "v_mfma_f32_16x16x32_bf16 v[40:43], a[216:219], a[144:147], v[40:43]\n"
    "buffer_load_dwordx4 v10, s[20:23], s33 offen lds\n"
    "v_mfma_f32_16x16x32_bf16 v[72:75], a[216:219], a[148:151], v[72:75]\n"
    "ds_read_b64_tr_b16 a[212:213], v3 offset:8456\n"
    "ds_read_b64_tr_b16 a[214:215], v3 offset:8968\n"
    "v_mfma_f32_16x16x32_bf16 v[104:107], a[216:219], a[152:155], v[104:107]\n"
    "buffer_load_dwordx4 a[196:199], v19, s[28:31], s46 offen offset:3072\n"
    "v_mfma_f32_16x16x32_bf16 v[136:139], a[216:219], a[156:159], v[136:139]\n"
    "s_waitcnt lgkmcnt(8)\n"
    "v_mfma_f32_16x16x32_bf16 v[44:47], a[220:223], a[144:147], v[44:47]\n"
    "v_mfma_f32_16x16x32_bf16 v[76:79], a[220:223], a[148:151], v[76:79]\n"
    "ds_read_b64_tr_b16 a[216:217], v3 offset:8512\n"
    "ds_read_b64_tr_b16 a[218:219], v3 offset:9024\n"
    "v_mfma_f32_16x16x32_bf16 v[108:111], a[220:223], a[152:155], v[108:111]\n"
    "v_mfma_f32_16x16x32_bf16 v[140:143], a[220:223], a[156:159], v[140:143]\n"
    "s_waitcnt lgkmcnt(8)\n"
    "v_mfma_f32_16x16x32_bf16 v[48:51], a[224:227], a[144:147], v[48:51]\n"
    "v_mfma_f32_16x16x32_bf16 v[80:83], a[224:227], a[148:151], v[80:83]\n"
    "ds_read_b64_tr_b16 a[220:221], v3 offset:8520\n"
    "ds_read_b64_tr_b16 a[222:223], v3 offset:9032\n"
    "v_mfma_f32_16x16x32_bf16 v[112:115], a[224:227], a[152:155], v[112:115]\n"
    "s_add_u32 m0, s40, s65\n"
    "buffer_load_dwordx4 a[200:203], v19, s[28:31], s47 offen offset:3072\n"
    "v_mfma_f32_16x16x32_bf16 v[144:147], a[224:227], a[156:159], v[144:147]\n"
    "s_waitcnt lgkmcnt(8)\n"
    "v_mfma_f32_16x16x32_bf16 v[52:55], a[228:231], a[144:147], v[52:55]\n"
    "buffer_load_dwordx4 v11, s[20:23], s33 offen lds\n"
    "v_mfma_f32_16x16x32_bf16 v[84:87], a[228:231], a[148:151], v[84:87]\n"
    "ds_read_b64_tr_b16 a[224:225], v3 offset:12672\n"
    "ds_read_b64_tr_b16 a[226:227], v3 offset:13184\n"
    "v_mfma_f32_16x16x32_bf16 v[116:119], a[228:231], a[152:155], v[116:119]\n"
    "v_mfma_f32_16x16x32_bf16 v[148:151], a[228:231], a[156:159], v[148:151]\n"
    "s_waitcnt lgkmcnt(8)\n"
    "v_mfma_f32_16x16x32_bf16 v[56:59], a[232:235], a[144:147], v[56:59]\n"
    "v_mfma_f32_16x16x32_bf16 v[88:91], a[232:235], a[148:151], v[88:91]\n"
    "ds_read_b64_tr_b16 a[228:229], v3 offset:12680\n"
    "ds_read_b64_tr_b16 a[230:231], v3 offset:13192\n"
    "v_mfma_f32_16x16x32_bf16 v[120:123], a[232:235], a[152:155], v[120:123]\n"
    "buffer_load_dwordx4 a[204:207], v19, s[28:31], s48 offen offset:3072\n"
    "v_mfma_f32_16x16x32_bf16 v[152:155], a[232:235], a[156:159], v[152:155]\n"
    "s_waitcnt lgkmcnt(8)\n"
    "v_mfma_f32_16x16x32_bf16 v[60:63], a[236:239], a[144:147], v[60:63]\n"
    "v_mfma_f32_16x16x32_bf16 v[92:95], a[236:239], a[148:151], v[92:95]\n"
    "ds_read_b64_tr_b16 a[232:233], v3 offset:12736\n"
    "ds_read_b64_tr_b16 a[234:235], v3 offset:13248\n"
    "v_mfma_f32_16x16x32_bf16 v[124:127], a[236:239], a[152:155], v[124:127]\n"
    "v_mov_b32 v19, v4\n"
    "v_mfma_f32_16x16x32_bf16 v[156:159], a[236:239], a[156:159], v[156:159]\n"
    "s_waitcnt lgkmcnt(8) vmcnt(11)\n"
    "s_add_u32 m0, s40, s66\n"
    "v_mfma_f32_16x16x32_bf16 v[32:35], a[208:211], a[160:163], v[32:35]\n"
    "buffer_load_dwordx4 v12, s[20:23], s33 offen lds\n"
    "v_mfma_f32_16x16x32_bf16 v[64:67], a[208:211], a[164:167], v[64:67]\n"
    "ds_read_b64_tr_b16 a[236:237], v3 offset:12744\n"
    "ds_read_b64_tr_b16 a[238:239], v3 offset:13256\n"
    "v_mfma_f32_16x16x32_bf16 v[96:99], a[208:211], a[168:171], v[96:99]\n"
    "buffer_load_dwordx4 a[144:147], v4, s[28:31], 0 offen offset:0\n"
    "v_mfma_f32_16x16x32_bf16 v[128:131], a[208:211], a[172:175], v[128:131]\n"
    "s_waitcnt lgkmcnt(8)\n"
    "v_mfma_f32_16x16x32_bf16 v[36:39], a[212:215], a[160:163], v[36:39]\n"
    "v_mfma_f32_16x16x32_bf16 v[68:71], a[212:215], a[164:167], v[68:71]\n"
    "ds_read_b64_tr_b16 a[208:209], v3 offset:16896\n"
    "ds_read_b64_tr_b16 a[210:211], v3 offset:17408\n"
    "v_mfma_f32_16x16x32_bf16 v[100:103], a[212:215], a[168:171], v[100:103]\n"
    "v_mfma_f32_16x16x32_bf16 v[132:135], a[212:215], a[172:175], v[132:135]\n"
    "s_waitcnt lgkmcnt(8)\n"
    "v_mfma_f32_16x16x32_bf16 v[40:43], a[216:219], a[160:163], v[40:43]\n"
    "s_add_u32 s49, s49, 4096\n"
    "v_mfma_f32_16x16x32_bf16 v[72:75], a[216:219], a[164:167], v[72:75]\n"
    "ds_read_b64_tr_b16 a[212:213], v3 offset:16904\n"
    "ds_read_b64_tr_b16 a[214:215], v3 offset:17416\n"
    "v_mfma_f32_16x16x32_bf16 v[104:107], a[216:219], a[168:171], v[104:107]\n"
    "buffer_load_dwordx4 a[148:151], v4, s[28:31], s46 offen offset:0\n"
    "v_mfma_f32_16x16x32_bf16 v[136:139], a[216:219], a[172:175], v[136:139]\n"
    "s_waitcnt lgkmcnt(8)\n"
    "v_mfma_f32_16x16x32_bf16 v[44:47], a[220:223], a[160:163], v[44:47]\n"
    "s_add_u32 m0, s40, s67\n"
    "v_mfma_f32_16x16x32_bf16 v[76:79], a[220:223], a[164:167], v[76:79]\n"
    "ds_read_b64_tr_b16 a[216:217], v3 offset:16960\n"
    "ds_read_b64_tr_b16 a[218:219], v3 offset:17472\n"
    "v_mfma_f32_16x16x32_bf16 v[108:111], a[220:223], a[168:171], v[108:111]\n"
    "buffer_load_dwordx4 v13, s[20:23], s33 offen lds\n"
    "v_mfma_f32_16x16x32_bf16 v[140:143], a[220:223], a[172:175], v[140:143]\n"
    "s_waitcnt lgkmcnt(8)\n"
    "v_mfma_f32_16x16x32_bf16 v[48:51], a[224:227], a[160:163], v[48:51]\n"
    "s_cmp_eq_u32 s49, 20480\n"
    "s_cselect_b32 s49, 0, s49\n"
    "v_mfma_f32_16x16x32_bf16 v[80:83], a[224:227], a[164:167], v[80:83]\n"
    "ds_read_b64_tr_b16 a[220:221], v3 offset:16968\n"
    "ds_read_b64_tr_b16 a[222:223], v3 offset:17480\n"
    "v_mfma_f32_16x16x32_bf16 v[112:115], a[224:227], a[168:171], v[112:115]\n"
    "buffer_load_dwordx4 a[152:155], v4, s[28:31], s47 offen offset:0\n"
    "v_mfma_f32_16x16x32_bf16 v[144:147], a[224:227], a[172:175], v[144:147]\n"
    "s_waitcnt lgkmcnt(8)\n"
    "v_mfma_f32_16x16x32_bf16 v[52:55], a[228:231], a[160:163], v[52:55]\n"
    "v_mfma_f32_16x16x32_bf16 v[84:87], a[228:231], a[164:167], v[84:87]\n"
    "ds_read_b64_tr_b16 a[224:225], v3 offset:21120\n"
    "ds_read_b64_tr_b16 a[226:227], v3 offset:21632\n"
    "v_mfma_f32_16x16x32_bf16 v[116:119], a[228:231], a[168:171], v[116:119]\n"
    "v_mfma_f32_16x16x32_bf16 v[148:151], a[228:231], a[172:175], v[148:151]\n"
    "s_waitcnt lgkmcnt(8)\n"
    "v_mfma_f32_16x16x32_bf16 v[56:59], a[232:235], a[160:163], v[56:59]\n"
    "v_mfma_f32_16x16x32_bf16 v[88:91], a[232:235], a[164:167], v[88:91]\n"
    "s_add_u32 m0, s40, s68\n"
    "ds_read_b64_tr_b16 a[228:229], v3 offset:21128\n"
    "v_mfma_f32_16x16x32_bf16 v[120:123], a[232:235], a[168:171], v[120:123]\n"
    "ds_read_b64_tr_b16 a[230:231], v3 offset:21640\n"
    "buffer_load_dwordx4 a[156:159], v4, s[28:31], s48 offen offset:0\n"
    "v_mfma_f32_16x16x32_bf16 v[152:155], a[232:235], a[172:175], v[152:155]\n"
    "s_waitcnt lgkmcnt(8)\n"
    "v_mfma_f32_16x16x32_bf16 v[60:63], a[236:239], a[160:163], v[60:63]\n"
    "buffer_load_dwordx4 v14, s[24:27], s33 offen lds\n"
    "v_mfma_f32_16x16x32_bf16 v[92:95], a[236:239], a[164:167], v[92:95]\n"
    "ds_read_b64_tr_b16 a[232:233], v3 offset:21184\n"
    "ds_read_b64_tr_b16 a[234:235], v3 offset:21696\n"
    "v_mfma_f32_16x16x32_bf16 v[124:127], a[236:239], a[168:171], v[124:127]\n"
    "v_mfma_f32_16x16x32_bf16 v[156:159], a[236:239], a[172:175], v[156:159]\n"
    "s_waitcnt vmcnt(13) lgkmcnt(8)\n"
    "v_mfma_f32_16x16x32_bf16 v[32:35], a[208:211], a[176:179], v[32:35]\n"
    "v_mfma_f32_16x16x32_bf16 v[64:67], a[208:211], a[180:183], v[64:67]\n"
    "ds_read_b64_tr_b16 a[236:237], v3 offset:21192\n"
    "ds_read_b64_tr_b16 a[238:239], v3 offset:21704\n"
    "v_mfma_f32_16x16x32_bf16 v[96:99], a[208:211], a[184:187], v[96:99]\n"
    "buffer_load_dwordx4 a[160:163], v4, s[28:31], 0 offen offset:1024\n"
    "v_mfma_f32_16x16x32_bf16 v[128:131], a[208:211], a[188:191], v[128:131]\n"
    "s_waitcnt lgkmcnt(8)\n"
    "v_mfma_f32_16x16x32_bf16 v[36:39], a[212:215], a[176:179], v[36:39]\n"
    "v_mfma_f32_16x16x32_bf16 v[68:71], a[212:215], a[180:183], v[68:71]\n"
    "ds_read_b64_tr_b16 a[208:209], v3 offset:25344\n"
    "ds_read_b64_tr_b16 a[210:211], v3 offset:25856\n"
    "v_mfma_f32_16x16x32_bf16 v[100:103], a[212:215], a[184:187], v[100:103]\n"
    "s_add_u32 m0, s40, s69\n"
    "v_mfma_f32_16x16x32_bf16 v[132:135], a[212:215], a[188:191], v[132:135]\n"
    "s_waitcnt lgkmcnt(8)\n"
    "v_mfma_f32_16x16x32_bf16 v[40:43], a[216:219], a[176:179], v[40:43]\n"
    "buffer_load_dwordx4 v15, s[24:27], s33 offen lds\n"
    "v_mfma_f32_16x16x32_bf16 v[72:75], a[216:219], a[180:183], v[72:75]\n"
    "ds_read_b64_tr_b16 a[212:213], v3 offset:25352\n"
    "ds_read_b64_tr_b16 a[214:215], v3 offset:25864\n"
    "v_mfma_f32_16x16x32_bf16 v[104:107], a[216:219], a[184:187], v[104:107]\n"
    "buffer_load_dwordx4 a[164:167], v4, s[28:31], s46 offen offset:1024\n"
    "v_mfma_f32_16x16x32_bf16 v[136:139], a[216:219], a[188:191], v[136:139]\n"
    "s_waitcnt lgkmcnt(8)\n"
    "v_mfma_f32_16x16x32_bf16 v[44:47], a[220:223], a[176:179], v[44:47]\n"
    "v_mfma_f32_16x16x32_bf16 v[76:79], a[220:223], a[180:183], v[76:79]\n"
    "ds_read_b64_tr_b16 a[216:217], v3 offset:25408\n"
    "ds_read_b64_tr_b16 a[218:219], v3 offset:25920\n"
    "v_mfma_f32_16x16x32_bf16 v[108:111], a[220:223], a[184:187], v[108:111]\n"
    "v_mfma_f32_16x16x32_bf16 v[140:143], a[220:223], a[188:191], v[140:143]\n"
    "s_waitcnt lgkmcnt(8)\n"
    "v_mfma_f32_16x16x32_bf16 v[48:51], a[224:227], a[176:179], v[48:51]\n"
    "v_mfma_f32_16x16x32_bf16 v[80:83], a[224:227], a[180:183], v[80:83]\n"
    "ds_read_b64_tr_b16 a[220:221], v3 offset:25416\n"
    "ds_read_b64_tr_b16 a[222:223], v3 offset:25928\n"
    "v_mfma_f32_16x16x32_bf16 v[112:115], a[224:227], a[184:187], v[112:115]\n"
    "buffer_load_dwordx4 a[168:171], v4, s[28:31], s47 offen offset:1024\n"
    "v_mfma_f32_16x16x32_bf16 v[144:147], a[224:227], a[188:191], v[144:147]\n"
    "s_waitcnt lgkmcnt(8)\n"
    "s_add_u32 m0, s40, s70\n"
    "v_mfma_f32_16x16x32_bf16 v[52:55], a[228:231], a[176:179], v[52:55]\n"
    "buffer_load_dwordx4 v16, s[24:27], s33 offen lds\n"
    "v_mfma_f32_16x16x32_bf16 v[84:87], a[228:231], a[180:183], v[84:87]\n"
    "ds_read_b64_tr_b16 a[224:225], v3 offset:29568\n"
    "ds_read_b64_tr_b16 a[226:227], v3 offset:30080\n"
    "v_mfma_f32_16x16x32_bf16 v[116:119], a[228:231], a[184:187], v[116:119]\n"
    "v_mfma_f32_16x16x32_bf16 v[148:151], a[228:231], a[188:191], v[148:151]\n"
    "s_waitcnt lgkmcnt(8)\n"
    "v_mfma_f32_16x16x32_bf16 v[56:59], a[232:235], a[176:179], v[56:59]\n"
    "v_mfma_f32_16x16x32_bf16 v[88:91], a[232:235], a[180:183], v[88:91]\n"
    "ds_read_b64_tr_b16 a[228:229], v3 offset:29576\n"
    "ds_read_b64_tr_b16 a[230:231], v3 offset:30088\n"
    "v_mfma_f32_16x16x32_bf16 v[120:123], a[232:235], a[184:187], v[120:123]\n"
    "buffer_load_dwordx4 a[172:175], v4, s[28:31], s48 offen offset:1024\n"
    "v_mfma_f32_16x16x32_bf16 v[152:155], a[232:235], a[188:191], v[152:155]\n"
    "v_mfma_f32_16x16x32_bf16 v[60:63], a[236:239], a[176:179], v[60:63]\n"
    "v_mfma_f32_16x16x32_bf16 v[92:95], a[236:239], a[180:183], v[92:95]\n"
    "ds_read_b64_tr_b16 a[232:233], v3 offset:29632\n"
    "ds_read_b64_tr_b16 a[234:235], v3 offset:30144\n"
    "v_mfma_f32_16x16x32_bf16 v[124:127], a[236:239], a[184:187], v[124:127]\n"
    "v_mfma_f32_16x16x32_bf16 v[156:159], a[236:239], a[188:191], v[156:159]\n"
    "s_waitcnt vmcnt(13) lgkmcnt(8)\n"
    "v_mfma_f32_16x16x32_bf16 v[32:35], a[208:211], a[192:195], v[32:35]\n"
    "s_add_u32 m0, s40, s71\n"
    "v_mfma_f32_16x16x32_bf16 v[64:67], a[208:211], a[196:199], v[64:67]\n"
    "ds_read_b64_tr_b16 a[236:237], v3 offset:29640\n"
    "ds_read_b64_tr_b16 a[238:239], v3 offset:30152\n"
    "v_mfma_f32_16x16x32_bf16 v[96:99], a[208:211], a[200:203], v[96:99]\n"
    "buffer_load_dwordx4 v17, s[24:27], s33 offen lds\n"
    "v_mfma_f32_16x16x32_bf16 v[128:131], a[208:211], a[204:207], v[128:131]\n"
    "s_barrier\n"
    "buffer_load_dwordx4 a[176:179], v4, s[28:31], 0 offen offset:2048\n"
    "v_mfma_f32_16x16x32_bf16 v[36:39], a[212:215], a[192:195], v[36:39]\n"
    "s_add_u32 s33, s33, s41\n"
    "s_add_u32 s40, s40, s51\n"
    "v_mfma_f32_16x16x32_bf16 v[68:71], a[212:215], a[196:199], v[68:71]\n"
    "v_add_u32 v3, s44, v3\n"
    "ds_read_b64_tr_b16 a[208:209], v3 offset:0\n"
    "v_mfma_f32_16x16x32_bf16 v[100:103], a[212:215], a[200:203], v[100:103]\n"
    "ds_read_b64_tr_b16 a[210:211], v3 offset:512\n"
    "v_mfma_f32_16x16x32_bf16 v[132:135], a[212:215], a[204:207], v[132:135]\n"
    "s_waitcnt lgkmcnt(8)\n"
    "v_mfma_f32_16x16x32_bf16 v[40:43], a[216:219], a[192:195], v[40:43]\n"
    "s_add_u32 s43, s43, s44\n"
    "buffer_load_dwordx4 a[180:183], v4, s[28:31], s46 offen offset:2048\n"
    "v_mfma_f32_16x16x32_bf16 v[72:75], a[216:219], a[196:199], v[72:75]\n"
    "ds_read_b64_tr_b16 a[212:213], v3 offset:8\n"
    "ds_read_b64_tr_b16 a[214:215], v3 offset:520\n"
    "v_mfma_f32_16x16x32_bf16 v[104:107], a[216:219], a[200:203], v[104:107]\n"
    "s_cmp_eq_u32 s43, 101376\n"
    "s_cselect_b32 s44, -101376, s51\n"
    "v_mfma_f32_16x16x32_bf16 v[136:139], a[216:219], a[204:207], v[136:139]\n"
    "s_waitcnt lgkmcnt(8)\n"
    "v_mfma_f32_16x16x32_bf16 v[44:47], a[220:223], a[192:195], v[44:47]\n"
    "s_cmp_eq_u32 s40, 135168\n"
    "s_cselect_b32 s40, 0, s40\n"
    "v_mfma_f32_16x16x32_bf16 v[76:79], a[220:223], a[196:199], v[76:79]\n"
    "ds_read_b64_tr_b16 a[216:217], v3 offset:64\n"
    "ds_read_b64_tr_b16 a[218:219], v3 offset:576\n"
    "v_mfma_f32_16x16x32_bf16 v[108:111], a[220:223], a[200:203], v[108:111]\n"
    "buffer_load_dwordx4 a[184:187], v4, s[28:31], s47 offen offset:2048\n"
    "v_mfma_f32_16x16x32_bf16 v[140:143], a[220:223], a[204:207], v[140:143]\n"
    "s_waitcnt lgkmcnt(8)\n"
    "v_mfma_f32_16x16x32_bf16 v[48:51], a[224:227], a[192:195], v[48:51]\n"
    "v_mfma_f32_16x16x32_bf16 v[80:83], a[224:227], a[196:199], v[80:83]\n"
    "ds_read_b64_tr_b16 a[220:221], v3 offset:72\n"
    "ds_read_b64_tr_b16 a[222:223], v3 offset:584\n"
    "v_mfma_f32_16x16x32_bf16 v[112:115], a[224:227], a[200:203], v[112:115]\n"
    "v_mfma_f32_16x16x32_bf16 v[144:147], a[224:227], a[204:207], v[144:147]\n"
    "s_waitcnt lgkmcnt(8)\n"
    "v_mfma_f32_16x16x32_bf16 v[52:55], a[228:231], a[192:195], v[52:55]\n"
    "buffer_load_dwordx4 a[188:191], v4, s[28:31], s48 offen offset:2048\n"
    "v_mfma_f32_16x16x32_bf16 v[84:87], a[228:231], a[196:199], v[84:87]\n"
    "ds_read_b64_tr_b16 a[224:225], v3 offset:4224\n"
    "ds_read_b64_tr_b16 a[226:227], v3 offset:4736\n"
    "v_mfma_f32_16x16x32_bf16 v[116:119], a[228:231], a[200:203], v[116:119]\n"
    "v_add_u32 v4, s49, v2\n"
    "v_mfma_f32_16x16x32_bf16 v[148:151], a[228:231], a[204:207], v[148:151]\n"
    "v_mfma_f32_16x16x32_bf16 v[56:59], a[232:235], a[192:195], v[56:59]\n"
    "v_mfma_f32_16x16x32_bf16 v[88:91], a[232:235], a[196:199], v[88:91]\n"
    "ds_read_b64_tr_b16 a[228:229], v3 offset:4232\n"
    "ds_read_b64_tr_b16 a[230:231], v3 offset:4744\n"
    "v_mfma_f32_16x16x32_bf16 v[120:123], a[232:235], a[200:203], v[120:123]\n"
    "v_mfma_f32_16x16x32_bf16 v[152:155], a[232:235], a[204:207], v[152:155]\n"
    "s_waitcnt lgkmcnt(8)\n"
    "v_mfma_f32_16x16x32_bf16 v[60:63], a[236:239], a[192:195], v[60:63]\n"
    "v_mfma_f32_16x16x32_bf16 v[92:95], a[236:239], a[196:199], v[92:95]\n"
    "v_mfma_f32_16x16x32_bf16 v[124:127], a[236:239], a[200:203], v[124:127]\n"
    "v_mfma_f32_16x16x32_bf16 v[156:159], a[236:239], a[204:207], v[156:159]\n"
    "s_cmp_lg_u32 s50, 2\n"
    "s_cbranch_scc1 K1_Q1\n"
    "v_mov_b32 v10, v164\n"
    "v_mov_b32 v11, v165\n"
    "v_mov_b32 v12, v166\n"
    "v_mov_b32 v13, v167\n"
    "v_mov_b32 v14, v168\n"
    "v_mov_b32 v15, v169\n"
    "v_mov_b32 v16, v170\n"
    "v_mov_b32 v17, v171\n"
    "s_mov_b64 s[20:21], s[84:85]\n"
    "s_mov_b64 s[24:25], s[86:87]\n"
    "s_mov_b32 s22, 122880\n"
    "s_mov_b32 s26, 131072\n"
    "s_mov_b32 s41, 16384\n"
    "s_mov_b32 s33, 0\n"
    "s_add_u32 s64, s45, 0\n"
    "s_add_u32 s65, s45, 4096\n"
    "s_add_u32 s66, s45, 8192\n"
    "s_add_u32 s67, s45, 12288\n"
    "s_add_u32 s68, s59, 16384\n"
    "s_add_u32 s69, s59, 24832\n"
    "s_add_u32 s70, s59, 20608\n"
    "s_add_u32 s71, s59, 29056\n"
    "K1_Q1:\n"
    "s_cmp_lg_u32 s50, 0\n"
    "s_cbranch_scc1 K1_Q\n"
    "s_mov_b32 s56, 0x3e0293ee\n"
    "s_mov_b32 s57, s56\n"
    "ds_read_b128 v[176:179], v172 offset:0\n"
    "ds_read_b128 v[180:183], v172 offset:4096\n"
    "ds_read_b128 v[184:187], v173 offset:0\n"
    "ds_read_b128 v[188:191], v173 offset:4096\n"
    "ds_read_b128 v[192:195], v174 offset:0\n"
    "ds_read_b128 v[196:199], v174 offset:4096\n"
    "ds_read_b128 v[200:203], v175 offset:0\n"
    "ds_read_b128 v[204:207], v175 offset:4096\n"
    "v_pk_mul_f32 v[32:33], v[32:33], s[56:57]\n"
    "v_pk_mul_f32 v[34:35], v[34:35], s[56:57]\n"
    "v_pk_mul_f32 v[36:37], v[36:37], s[56:57]\n"
    "v_pk_mul_f32 v[38:39], v[38:39], s[56:57]\n"
    "v_cvt_pk_bf16_f32 v32, v32, v33\n"
    "v_cvt_pk_bf16_f32 v33, v34, v35\n"
    "v_cvt_pk_bf16_f32 v34, v36, v37\n"
    "v_cvt_pk_bf16_f32 v35, v38, v39\n"
    "v_pk_mul_f32 v[40:41], v[40:41], s[56:57]\n"
    "v_pk_mul_f32 v[42:43], v[42:43], s[56:57]\n"
    "v_pk_mul_f32 v[44:45], v[44:45], s[56:57]\n"
    "v_pk_mul_f32 v[46:47], v[46:47], s[56:57]\n"
    "v_cvt_pk_bf16_f32 v36, v40, v41\n"
    "v_cvt_pk_bf16_f32 v37, v42, v43\n"
    "v_cvt_pk_bf16_f32 v38, v44, v45\n"
    "v_cvt_pk_bf16_f32 v39, v46, v47\n"
    "v_pk_mul_f32 v[48:49], v[48:49], s[56:57]\n"
    "v_pk_mul_f32 v[50:51], v[50:51], s[56:57]\n"
    "v_pk_mul_f32 v[52:53], v[52:53], s[56:57]\n"
    "v_pk_mul_f32 v[54:55], v[54:55], s[56:57]\n"
    "v_cvt_pk_bf16_f32 v40, v48, v49\n"
    "v_cvt_pk_bf16_f32 v41, v50, v51\n"
    "v_cvt_pk_bf16_f32 v42, v52, v53\n"
    "v_cvt_pk_bf16_f32 v43, v54, v55\n"
    "v_pk_mul_f32 v[56:57], v[56:57], s[56:57]\n"
    "v_pk_mul_f32 v[58:59], v[58:59], s[56:57]\n"
    "v_pk_mul_f32 v[60:61], v[60:61], s[56:57]\n"
    "v_pk_mul_f32 v[62:63], v[62:63], s[56:57]\n"
    "v_cvt_pk_bf16_f32 v44, v56, v57\n"
    "v_cvt_pk_bf16_f32 v45, v58, v59\n"
    "v_cvt_pk_bf16_f32 v46, v60, v61\n"
    "v_cvt_pk_bf16_f32 v47, v62, v63\n"
    "v_pk_mul_f32 v[64:65], v[64:65], s[56:57]\n"
    "v_pk_mul_f32 v[66:67], v[66:67], s[56:57]\n"
    "v_pk_mul_f32 v[68:69], v[68:69], s[56:57]\n"
    "v_pk_mul_f32 v[70:71], v[70:71], s[56:57]\n"
    "v_cvt_pk_bf16_f32 v48, v64, v65\n"
    "v_cvt_pk_bf16_f32 v49, v66, v67\n"
    "v_cvt_pk_bf16_f32 v50, v68, v69\n"
    "v_cvt_pk_bf16_f32 v51, v70, v71\n"
    "v_pk_mul_f32 v[72:73], v[72:73], s[56:57]\n"
    "v_pk_mul_f32 v[74:75], v[74:75], s[56:57]\n"
    "v_pk_mul_f32 v[76:77], v[76:77], s[56:57]\n"
    "v_pk_mul_f32 v[78:79], v[78:79], s[56:57]\n"
    "v_cvt_pk_bf16_f32 v52, v72, v73\n"
    "v_cvt_pk_bf16_f32 v53, v74, v75\n"
    "v_cvt_pk_bf16_f32 v54, v76, v77\n"
    "v_cvt_pk_bf16_f32 v55, v78, v79\n"
    "v_pk_mul_f32 v[80:81], v[80:81], s[56:57]\n"
    "v_pk_mul_f32 v[82:83], v[82:83], s[56:57]\n"
    "v_pk_mul_f32 v[84:85], v[84:85], s[56:57]\n"
    "v_pk_mul_f32 v[86:87], v[86:87], s[56:57]\n"
    "v_cvt_pk_bf16_f32 v56, v80, v81\n"
    "v_cvt_pk_bf16_f32 v57, v82, v83\n"
    "v_cvt_pk_bf16_f32 v58, v84, v85\n"
    "v_cvt_pk_bf16_f32 v59, v86, v87\n"
    "v_pk_mul_f32 v[88:89], v[88:89], s[56:57]\n"
    "v_pk_mul_f32 v[90:91], v[90:91], s[56:57]\n"
    "v_pk_mul_f32 v[92:93], v[92:93], s[56:57]\n"
    "v_pk_mul_f32 v[94:95], v[94:95], s[56:57]\n"
    "v_cvt_pk_bf16_f32 v60, v88, v89\n"
    "v_cvt_pk_bf16_f32 v61, v90, v91\n"
    "v_cvt_pk_bf16_f32 v62, v92, v93\n"
    "v_cvt_pk_bf16_f32 v63, v94, v95\n"
    "v_pk_mul_f32 v[96:97], v[96:97], s[56:57]\n"
    "v_pk_mul_f32 v[98:99], v[98:99], s[56:57]\n"
    "v_pk_mul_f32 v[100:101], v[100:101], s[56:57]\n"
    "v_pk_mul_f32 v[102:103], v[102:103], s[56:57]\n"
    "v_cvt_pk_bf16_f32 v64, v96, v97\n"
    "v_cvt_pk_bf16_f32 v65, v98, v99\n"
    "v_cvt_pk_bf16_f32 v66, v100, v101\n"
    "v_cvt_pk_bf16_f32 v67, v102, v103\n"
    "v_pk_mul_f32 v[104:105], v[104:105], s[56:57]\n"
    "v_pk_mul_f32 v[106:107], v[106:107], s[56:57]\n"
    "v_pk_mul_f32 v[108:109], v[108:109], s[56:57]\n"
    "v_pk_mul_f32 v[110:111], v[110:111], s[56:57]\n"
    "v_cvt_pk_bf16_f32 v68, v104, v105\n"
    "v_cvt_pk_bf16_f32 v69, v106, v107\n"
    "v_cvt_pk_bf16_f32 v70, v108, v109\n"
    "v_cvt_pk_bf16_f32 v71, v110, v111\n"
    "v_pk_mul_f32 v[112:113], v[112:113], s[56:57]\n"
    "v_pk_mul_f32 v[114:115], v[114:115], s[56:57]\n"
    "v_pk_mul_f32 v[116:117], v[116:117], s[56:57]\n"
    "v_pk_mul_f32 v[118:119], v[118:119], s[56:57]\n"
    "v_cvt_pk_bf16_f32 v72, v112, v113\n"
    "v_cvt_pk_bf16_f32 v73, v114, v115\n"
    "v_cvt_pk_bf16_f32 v74, v116, v117\n"
    "v_cvt_pk_bf16_f32 v75, v118, v119\n"
    "v_pk_mul_f32 v[120:121], v[120:121], s[56:57]\n"
    "v_pk_mul_f32 v[122:123], v[122:123], s[56:57]\n"
    "v_pk_mul_f32 v[124:125], v[124:125], s[56:57]\n"
    "v_pk_mul_f32 v[126:127], v[126:127], s[56:57]\n"
    "v_cvt_pk_bf16_f32 v76, v120, v121\n"
    "v_cvt_pk_bf16_f32 v77, v122, v123\n"
    "v_cvt_pk_bf16_f32 v78, v124, v125\n"
    "v_cvt_pk_bf16_f32 v79, v126, v127\n"
    "v_pk_mul_f32 v[128:129], v[128:129], s[56:57]\n"
    "v_pk_mul_f32 v[130:131], v[130:131], s[56:57]\n"
    "v_pk_mul_f32 v[132:133], v[132:133], s[56:57]\n"
    "v_pk_mul_f32 v[134:135], v[134:135], s[56:57]\n"
    "v_cvt_pk_bf16_f32 v80, v128, v129\n"
    "v_cvt_pk_bf16_f32 v81, v130, v131\n"
    "v_cvt_pk_bf16_f32 v82, v132, v133\n"
    "v_cvt_pk_bf16_f32 v83, v134, v135\n"
    "v_pk_mul_f32 v[136:137], v[136:137], s[56:57]\n"
    "v_pk_mul_f32 v[138:139], v[138:139], s[56:57]\n"
    "v_pk_mul_f32 v[140:141], v[140:141], s[56:57]\n"
    "v_pk_mul_f32 v[142:143], v[142:143], s[56:57]\n"
    "v_cvt_pk_bf16_f32 v84, v136, v137\n"
    "v_cvt_pk_bf16_f32 v85, v138, v139\n"
    "v_cvt_pk_bf16_f32 v86, v140, v141\n"
    "v_cvt_pk_bf16_f32 v87, v142, v143\n"
    "v_pk_mul_f32 v[144:145], v[144:145], s[56:57]\n"
    "v_pk_mul_f32 v[146:147], v[146:147], s[56:57]\n"
    "v_pk_mul_f32 v[148:149], v[148:149], s[56:57]\n"
    "v_pk_mul_f32 v[150:151], v[150:151], s[56:57]\n"
    "v_cvt_pk_bf16_f32 v88, v144, v145\n"
    "v_cvt_pk_bf16_f32 v89, v146, v147\n"
    "v_cvt_pk_bf16_f32 v90, v148, v149\n"
    "v_cvt_pk_bf16_f32 v91, v150, v151\n"
    "v_pk_mul_f32 v[152:153], v[152:153], s[56:57]\n"
    "v_pk_mul_f32 v[154:155], v[154:155], s[56:57]\n"
    "v_pk_mul_f32 v[156:157], v[156:157], s[56:57]\n"
    "v_pk_mul_f32 v[158:159], v[158:159], s[56:57]\n"
    "v_cvt_pk_bf16_f32 v92, v152, v153\n"
    "v_cvt_pk_bf16_f32 v93, v154, v155\n"
    "v_cvt_pk_bf16_f32 v94, v156, v157\n"
    "v_cvt_pk_bf16_f32 v95, v158, v159\n"
    "s_waitcnt lgkmcnt(7)\n"
    "v_mfma_f32_16x16x32_bf16 v[96:99], v[176:179], v[32:35], 0\n"
    "v_mfma_f32_16x16x32_bf16 v[104:107], v[176:179], v[48:51], 0\n"
    "v_mfma_f32_16x16x32_bf16 v[112:115], v[176:179], v[64:67], 0\n"
    "v_mfma_f32_16x16x32_bf16 v[120:123], v[176:179], v[80:83], 0\n"
    "s_waitcnt lgkmcnt(6)\n"
    "v_mfma_f32_16x16x32_bf16 v[100:103], v[180:183], v[32:35], 0\n"
    "v_mfma_f32_16x16x32_bf16 v[108:111], v[180:183], v[48:51], 0\n"
    "v_mfma_f32_16x16x32_bf16 v[116:119], v[180:183], v[64:67], 0\n"
    "v_mfma_f32_16x16x32_bf16 v[124:127], v[180:183], v[80:83], 0\n"
    "s_waitcnt lgkmcnt(5)\n"
    "v_mfma_f32_16x16x32_bf16 v[96:99], v[184:187], v[36:39], v[96:99]\n"
    "v_mfma_f32_16x16x32_bf16 v[104:107], v[184:187], v[52:55], v[104:107]\n"
    "v_mfma_f32_16x16x32_bf16 v[112:115], v[184:187], v[68:71], v[112:115]\n"
    "v_mfma_f32_16x16x32_bf16 v[120:123], v[184:187], v[84:87], v[120:123]\n"
    "s_waitcnt lgkmcnt(2)\n"
    "v_mfma_f32_16x16x32_bf16 v[100:103], v[188:191], v[36:39], v[100:103]\n"
    "v_mfma_f32_16x16x32_bf16 v[108:111], v[188:191], v[52:55], v[108:111]\n"
    "v_mfma_f32_16x16x32_bf16 v[116:119], v[188:191], v[68:71], v[116:119]\n"
    "v_mfma_f32_16x16x32_bf16 v[124:127], v[188:191], v[84:87], v[124:127]\n"
    "s_waitcnt lgkmcnt(1)\n"
    "v_mfma_f32_16x16x32_bf16 v[96:99], v[192:195], v[40:43], v[96:99]\n"
    "v_mfma_f32_16x16x32_bf16 v[104:107], v[192:195], v[56:59], v[104:107]\n"
    "v_mfma_f32_16x16x32_bf16 v[112:115], v[192:195], v[72:75], v[112:115]\n"
    "v_mfma_f32_16x16x32_bf16 v[120:123], v[192:195], v[88:91], v[120:123]\n"
    "s_waitcnt lgkmcnt(0)\n"
    "v_mfma_f32_16x16x32_bf16 v[100:103], v[196:199], v[40:43], v[100:103]\n"
    "v_mfma_f32_16x16x32_bf16 v[108:111], v[196:199], v[56:59], v[108:111]\n"
    "v_mfma_f32_16x16x32_bf16 v[116:119], v[196:199], v[72:75], v[116:119]\n"
    "v_mfma_f32_16x16x32_bf16 v[124:127], v[196:199], v[88:91], v[124:127]\n"
    "v_mfma_f32_16x16x32_bf16 v[96:99], v[200:203], v[44:47], v[96:99]\n"
    "v_mfma_f32_16x16x32_bf16 v[104:107], v[200:203], v[60:63], v[104:107]\n"
    "v_mfma_f32_16x16x32_bf16 v[112:115], v[200:203], v[76:79], v[112:115]\n"
    "v_mfma_f32_16x16x32_bf16 v[120:123], v[200:203], v[92:95], v[120:123]\n"
    "v_mfma_f32_16x16x32_bf16 v[100:103], v[204:207], v[44:47], v[100:103]\n"
    "v_mfma_f32_16x16x32_bf16 v[108:111], v[204:207], v[60:63], v[108:111]\n"
    "v_mfma_f32_16x16x32_bf16 v[116:119], v[204:207], v[76:79], v[116:119]\n"
    "v_mfma_f32_16x16x32_bf16 v[124:127], v[204:207], v[92:95], v[124:127]\n"
    "ds_read_b128 v[176:179], v6 offset:0\n"
    "ds_read_b128 v[180:183], v6 offset:4096\n"
    "ds_read_b128 v[184:187], v7 offset:0\n"
    "ds_read_b128 v[188:191], v7 offset:4096\n"
    "ds_read_b128 v[192:195], v8 offset:0\n"
    "ds_read_b128 v[196:199], v8 offset:4096\n"
    "ds_read_b128 v[200:203], v9 offset:0\n"
    "ds_read_b128 v[204:207], v9 offset:4096\n"
    "v_exp_f32 v96, v96\n"
    "v_exp_f32 v97, v97\n"
    "v_exp_f32 v98, v98\n"
    "v_exp_f32 v99, v99\n"
    "v_exp_f32 v100, v100\n"
    "v_exp_f32 v101, v101\n"
    "v_exp_f32 v102, v102\n"
    "v_exp_f32 v103, v103\n"
    "v_exp_f32 v104, v104\n"
    "v_exp_f32 v105, v105\n"
    "v_exp_f32 v106, v106\n"
    "v_exp_f32 v107, v107\n"
    "v_exp_f32 v108, v108\n"
    "v_exp_f32 v109, v109\n"
    "v_exp_f32 v110, v110\n"
    "v_exp_f32 v111, v111\n"
    "v_cvt_pk_bf16_f32 v96, v96, v97\n"
    "v_cvt_pk_bf16_f32 v97, v98, v99\n"
    "v_cvt_pk_bf16_f32 v98, v100, v101\n"
    "v_cvt_pk_bf16_f32 v99, v102, v103\n"
    "v_cvt_pk_bf16_f32 v104, v104, v105\n"
    "v_cvt_pk_bf16_f32 v105, v106, v107\n"
    "v_cvt_pk_bf16_f32 v106, v108, v109\n"
    "v_cvt_pk_bf16_f32 v107, v110, v111\n"
    "s_mov_b32 s50, 8\n"
    "K1_AT:\n"
    "s_waitcnt lgkmcnt(7)\n"
    "v_mfma_f32_16x16x32_bf16 v[128:131], v[176:179], v[32:35], 0\n"
    "ds_read_b64_tr_b16 v[208:209], v5 offset:16384\n"
    "v_mfma_f32_16x16x32_bf16 v[136:139], v[176:179], v[48:51], 0\n"
    "v_exp_f32 v112, v112\n"
    "v_mfma_f32_16x16x32_bf16 v[144:147], v[176:179], v[64:67], 0\n"
    "v_exp_f32 v113, v113\n"
    "v_mfma_f32_16x16x32_bf16 v[152:155], v[176:179], v[80:83], 0\n"
    "s_waitcnt lgkmcnt(5)\n"
    "ds_read_b64_tr_b16 v[210:211], v5 offset:16896\n"
    "v_mfma_f32_16x16x32_bf16 v[132:135], v[180:183], v[32:35], 0\n"
    "v_exp_f32 v114, v114\n"
    "v_mfma_f32_16x16x32_bf16 v[140:143], v[180:183], v[48:51], 0\n"
    "ds_read_b64_tr_b16 v[212:213], v5 offset:16416\n"
    "v_cvt_pk_bf16_f32 v112, v112, v113\n"
    "v_mfma_f32_16x16x32_bf16 v[148:151], v[180:183], v[64:67], 0\n"
    "v_exp_f32 v115, v115\n"
    "v_mfma_f32_16x16x32_bf16 v[156:159], v[180:183], v[80:83], 0\n"
    "s_waitcnt lgkmcnt(6)\n"
    "ds_read_b64_tr_b16 v[214:215], v5 offset:16928\n"
    "v_mfma_f32_16x16x32_bf16 v[128:131], v[184:187], v[36:39], v[128:131]\n"
    "v_exp_f32 v116, v116\n"
    "v_mfma_f32_16x16x32_bf16 v[136:139], v[184:187], v[52:55], v[136:139]\n"
    "ds_read_b64_tr_b16 v[216:217], v5 offset:16448\n"
    "v_cvt_pk_bf16_f32 v113, v114, v115\n"
    "v_mfma_f32_16x16x32_bf16 v[144:147], v[184:187], v[68:71], v[144:147]\n"
    "v_exp_f32 v117, v117\n"
    "v_mfma_f32_16x16x32_bf16 v[152:155], v[184:187], v[84:87], v[152:155]\n"
    "s_waitcnt lgkmcnt(7)\n"
    "v_exp_f32 v118, v118\n"
    "v_mfma_f32_16x16x32_bf16 v[132:135], v[188:191], v[36:39], v[132:135]\n"
    "ds_read_b64_tr_b16 v[218:219], v5 offset:16960\n"
    "v_cvt_pk_bf16_f32 v114, v116, v117\n"
    "v_mfma_f32_16x16x32_bf16 v[140:143], v[188:191], v[52:55], v[140:143]\n"
    "v_exp_f32 v119, v119\n"
    "v_mfma_f32_16x16x32_bf16 v[148:151], v[188:191], v[68:71], v[148:151]\n"
    "buffer_load_dwordx4 v[244:247], v29, s[80:83], 0 offen offset:0\n"
    "v_cvt_pk_bf16_f32 v115, v118, v119\n"
    "v_mfma_f32_16x16x32_bf16 v[156:159], v[188:191], v[84:87], v[156:159]\n"
    "s_waitcnt lgkmcnt(7)\n"
    "ds_read_b64_tr_b16 v[220:221], v5 offset:16480\n"
    "v_mfma_f32_16x16x32_bf16 v[128:131], v[192:195], v[40:43], v[128:131]\n"
    "v_exp_f32 v120, v120\n"
    "v_mfma_f32_16x16x32_bf16 v[136:139], v[192:195], v[56:59], v[136:139]\n"
    "v_exp_f32 v121, v121\n"
    "v_mfma_f32_16x16x32_bf16 v[144:147], v[192:195], v[72:75], v[144:147]\n"
    "v_exp_f32 v122, v122\n"
    "v_mfma_f32_16x16x32_bf16 v[152:155], v[192:195], v[88:91], v[152:155]\n"
    "s_waitcnt lgkmcnt(7)\n"
    "v_cvt_pk_bf16_f32 v120, v120, v121\n"
    "v_mfma_f32_16x16x32_bf16 v[132:135], v[196:199], v[40:43], v[132:135]\n"
    "v_exp_f32 v123, v123\n"
    "v_mfma_f32_16x16x32_bf16 v[140:143], v[196:199], v[56:59], v[140:143]\n"
    "v_exp_f32 v124, v124\n"
    "v_mfma_f32_16x16x32_bf16 v[148:151], v[196:199], v[72:75], v[148:151]\n"
    "ds_read_b64_tr_b16 v[222:223], v5 offset:16992\n"
    "v_cvt_pk_bf16_f32 v121, v122, v123\n"
    "v_mfma_f32_16x16x32_bf16 v[156:159], v[196:199], v[88:91], v[156:159]\n"
    "s_waitcnt lgkmcnt(6)\n"
    "ds_read_b64_tr_b16 v[224:225], v5 offset:20608\n"
    "v_mfma_f32_16x16x32_bf16 v[128:131], v[200:203], v[44:47], v[128:131]\n"
    "v_exp_f32 v125, v125\n"
    "v_mfma_f32_16x16x32_bf16 v[136:139], v[200:203], v[60:63], v[136:139]\n"
    "v_exp_f32 v126, v126\n"
    "v_mfma_f32_16x16x32_bf16 v[144:147], v[200:203], v[76:79], v[144:147]\n"
    "ds_read_b64_tr_b16 v[226:227], v5 offset:21120\n"
    "v_cvt_pk_bf16_f32 v122, v124, v125\n"
    "v_mfma_f32_16x16x32_bf16 v[152:155], v[200:203], v[92:95], v[152:155]\n"
    "s_waitcnt lgkmcnt(6)\n"
    "ds_read_b64_tr_b16 v[228:229], v5 offset:20640\n"
    "v_mfma_f32_16x16x32_bf16 v[132:135], v[204:207], v[44:47], v[132:135]\n"
    "v_exp_f32 v127, v127\n"
    "v_mfma_f32_16x16x32_bf16 v[140:143], v[204:207], v[60:63], v[140:143]\n"
    "ds_read_b64_tr_b16 v[230:231], v5 offset:21152\n"
    "v_cvt_pk_bf16_f32 v123, v126, v127\n"
    "v_mfma_f32_16x16x32_bf16 v[148:151], v[204:207], v[76:79], v[148:151]\n"
    "ds_read_b64_tr_b16 v[232:233], v5 offset:20672\n"
    "ds_read_b64_tr_b16 v[234:235], v5 offset:21184\n"
    "v_mfma_f32_16x16x32_bf16 v[156:159], v[204:207], v[92:95], v[156:159]\n"
    "s_waitcnt lgkmcnt(8)\n"
    "ds_read_b64_tr_b16 v[236:237], v5 offset:20704\n"
    "v_mfma_f32_16x16x32_bf16 a[0:3], v[208:211], v[96:99], a[0:3]\n"
    "v_exp_f32 v128, v128\n"
    "v_mfma_f32_16x16x32_bf16 a[32:35], v[208:211], v[104:107], a[32:35]\n"
    "v_exp_f32 v129, v129\n"
    "v_mfma_f32_16x16x32_bf16 a[64:67], v[208:211], v[112:115], a[64:67]\n"
    "s_add_u32 m0, s40, s64\n"
    "ds_read_b64_tr_b16 v[238:239], v5 offset:21216\n"
    "v_mfma_f32_16x16x32_bf16 a[96:99], v[208:211], v[120:123], a[96:99]\n"
    "s_waitcnt lgkmcnt(8)\n"
    "ds_read_b128 v[176:179], v6 offset:8192\n"
    "v_mfma_f32_16x16x32_bf16 a[4:7], v[212:215], v[96:99], a[4:7]\n"
    "buffer_load_dwordx4 v10, s[20:23], s33 offen lds\n"
    "v_mfma_f32_16x16x32_bf16 a[36:39], v[212:215], v[104:107], a[36:39]\n"
    "v_exp_f32 v130, v130\n"
    "v_mfma_f32_16x16x32_bf16 a[68:71], v[212:215], v[112:115], a[68:71]\n"
    "ds_read_b128 v[180:183], v6 offset:12288\n"
    "v_cvt_pk_bf16_f32 v128, v128, v129\n"
    "v_mfma_f32_16x16x32_bf16 a[100:103], v[212:215], v[120:123], a[100:103]\n"
    "s_waitcnt lgkmcnt(8)\n"
    "ds_read_b128 v[184:187], v7 offset:8192\n"
    "v_mfma_f32_16x16x32_bf16 a[8:11], v[216:219], v[96:99], a[8:11]\n"
    "v_exp_f32 v131, v131\n"
    "v_mfma_f32_16x16x32_bf16 a[40:43], v[216:219], v[104:107], a[40:43]\n"
    "v_exp_f32 v132, v132\n"
    "v_mfma_f32_16x16x32_bf16 a[72:75], v[216:219], v[112:115], a[72:75]\n"
    "s_add_u32 m0, s40, s65\n"
    "v_cvt_pk_bf16_f32 v129, v130, v131\n"
    "v_mfma_f32_16x16x32_bf16 a[104:107], v[216:219], v[120:123], a[104:107]\n"
    "s_waitcnt lgkmcnt(7)\n"
    "ds_read_b128 v[188:191], v7 offset:12288\n"
    "v_mfma_f32_16x16x32_bf16 a[12:15], v[220:223], v[96:99], a[12:15]\n"
    "buffer_load_dwordx4 v11, s[20:23], s33 offen lds\n"
    "v_mfma_f32_16x16x32_bf16 a[44:47], v[220:223], v[104:107], a[44:47]\n"
    "v_exp_f32 v133, v133\n"
    "v_mfma_f32_16x16x32_bf16 a[76:79], v[220:223], v[112:115], a[76:79]\n"
    "v_exp_f32 v134, v134\n"
    "v_mfma_f32_16x16x32_bf16 a[108:111], v[220:223], v[120:123], a[108:111]\n"
    "s_waitcnt lgkmcnt(6)\n"
    "v_cvt_pk_bf16_f32 v130, v132, v133\n"
    "v_mfma_f32_16x16x32_bf16 a[16:19], v[224:227], v[96:99], a[16:19]\n"
    "v_exp_f32 v135, v135\n"
    "v_mfma_f32_16x16x32_bf16 a[48:51], v[224:227], v[104:107], a[48:51]\n"
    "ds_read_b128 v[192:195], v8 offset:8192\n"
    "v_cvt_pk_bf16_f32 v131, v134, v135\n"
    "v_mfma_f32_16x16x32_bf16 a[80:83], v[224:227], v[112:115], a[80:83]\n"
    "s_add_u32 m0, s40, s66\n"
    "ds_read_b128 v[196:199], v8 offset:12288\n"
    "v_mfma_f32_16x16x32_bf16 a[112:115], v[224:227], v[120:123], a[112:115]\n"
    "s_waitcnt lgkmcnt(6)\n"
    "ds_read_b128 v[200:203], v9 offset:8192\n"
    "v_mfma_f32_16x16x32_bf16 a[20:23], v[228:231], v[96:99], a[20:23]\n"
    "buffer_load_dwordx4 v12, s[20:23], s33 offen lds\n"
    "v_mfma_f32_16x16x32_bf16 a[52:55], v[228:231], v[104:107], a[52:55]\n"
    "v_exp_f32 v136, v136\n"
    "v_mfma_f32_16x16x32_bf16 a[84:87], v[228:231], v[112:115], a[84:87]\n"
    "v_exp_f32 v137, v137\n"
    "v_mfma_f32_16x16x32_bf16 a[116:119], v[228:231], v[120:123], a[116:119]\n"
    "v_exp_f32 v138, v138\n"
    "v_mfma_f32_16x16x32_bf16 a[24:27], v[232:235], v[96:99], a[24:27]\n"
    "ds_read_b128 v[204:207], v9 offset:12288\n"
    "v_cvt_pk_bf16_f32 v136, v136, v137\n"
    "v_mfma_f32_16x16x32_bf16 a[56:59], v[232:235], v[104:107], a[56:59]\n"
    "s_sub_u32 s50, s50, 1\n"
    "ds_read_b64_tr_b16 v[208:209], v5 offset:24832\n"
    "v_mfma_f32_16x16x32_bf16 a[88:91], v[232:235], v[112:115], a[88:91]\n"
    "v_exp_f32 v139, v139\n"
    "v_mfma_f32_16x16x32_bf16 a[120:123], v[232:235], v[120:123], a[120:123]\n"
    "s_waitcnt lgkmcnt(8)\n"
    "s_add_u32 m0, s40, s67\n"
    "v_mfma_f32_16x16x32_bf16 a[28:31], v[236:239], v[96:99], a[28:31]\n"
    "v_exp_f32 v140, v140\n"
    "v_mfma_f32_16x16x32_bf16 a[60:63], v[236:239], v[104:107], a[60:63]\n"
    "ds_read_b64_tr_b16 v[210:211], v5 offset:25344\n"
    "v_cvt_pk_bf16_f32 v137, v138, v139\n"
    "v_mfma_f32_16x16x32_bf16 a[92:95], v[236:239], v[112:115], a[92:95]\n"
    "v_exp_f32 v141, v141\n"
    "v_mfma_f32_16x16x32_bf16 a[124:127], v[236:239], v[120:123], a[124:127]\n"
    "buffer_load_dwordx4 v13, s[20:23], s33 offen lds\n"
    "v_mfma_f32_16x16x32_bf16 a[128:131], a[240:243], v[96:99], a[128:131]\n"
    "v_exp_f32 v142, v142\n"
    "v_mfma_f32_16x16x32_bf16 a[132:135], a[240:243], v[104:107], a[132:135]\n"
    "ds_read_b64_tr_b16 v[212:213], v5 offset:24864\n"
    "v_cvt_pk_bf16_f32 v138, v140, v141\n"
    "v_mfma_f32_16x16x32_bf16 a[136:139], a[240:243], v[112:115], a[136:139]\n"
    "v_exp_f32 v143, v143\n"
    "v_mfma_f32_16x16x32_bf16 a[140:143], a[240:243], v[120:123], a[140:143]\n"
    "s_waitcnt lgkmcnt(8)\n"
    "v_cvt_pk_bf16_f32 v139, v142, v143\n"
    "v_mfma_f32_16x16x32_bf16 v[96:99], v[176:179], v[32:35], 0\n"
    "s_add_u32 m0, s40, s68\n"
    "ds_read_b64_tr_b16 v[214:215], v5 offset:25376\n"
    "v_mfma_f32_16x16x32_bf16 v[104:107], v[176:179], v[48:51], 0\n"
    "v_exp_f32 v144, v144\n"
    "v_mfma_f32_16x16x32_bf16 v[112:115], v[176:179], v[64:67], 0\n"
    "buffer_load_dwordx4 v14, s[24:27], s33 offen lds\n"
    "v_mfma_f32_16x16x32_bf16 v[120:123], v[176:179], v[80:83], 0\n"
    "s_waitcnt lgkmcnt(8)\n"
    "buffer_load_dwordx4 v[244:247], v29, s[80:83], 0 offen offset:1024\n"
    "v_mfma_f32_16x16x32_bf16 v[100:103], v[180:183], v[32:35], 0\n"
    "v_exp_f32 v145, v145\n"
    "v_mfma_f32_16x16x32_bf16 v[108:111], v[180:183], v[48:51], 0\n"
    "v_exp_f32 v146, v146\n"
    "v_mfma_f32_16x16x32_bf16 v[116:119], v[180:183], v[64:67], 0\n"
    "s_add_u32 m0, s40, s69\n"
    "v_cvt_pk_bf16_f32 v144, v144, v145\n"
    "v_mfma_f32_16x16x32_bf16 v[124:127], v[180:183], v[80:83], 0\n"
    "s_waitcnt lgkmcnt(7)\n"
    "v_exp_f32 v147, v147\n"
    "v_mfma_f32_16x16x32_bf16 v[96:99], v[184:187], v[36:39], v[96:99]\n"
    "v_exp_f32 v148, v148\n"
    "v_mfma_f32_16x16x32_bf16 v[104:107], v[184:187], v[52:55], v[104:107]\n"
    "v_cvt_pk_bf16_f32 v145, v146, v147\n"
    "v_add_u32 v6, s44, v6\n"
    "v_mfma_f32_16x16x32_bf16 v[112:115], v[184:187], v[68:71], v[112:115]\n"
    "v_exp_f32 v149, v149\n"
    "v_mfma_f32_16x16x32_bf16 v[120:123], v[184:187], v[84:87], v[120:123]\n"
    "s_waitcnt lgkmcnt(6)\n"
    "buffer_load_dwordx4 v15, s[24:27], s33 offen lds\n"
    "v_exp_f32 v150, v150\n"
    "v_mfma_f32_16x16x32_bf16 v[100:103], v[188:191], v[36:39], v[100:103]\n"
    "v_cvt_pk_bf16_f32 v146, v148, v149\n"
    "v_add_u32 v7, s44, v7\n"
    "v_mfma_f32_16x16x32_bf16 v[108:111], v[188:191], v[52:55], v[108:111]\n"
    "v_exp_f32 v151, v151\n"
    "v_mfma_f32_16x16x32_bf16 v[116:119], v[188:191], v[68:71], v[116:119]\n"
    "s_add_u32 m0, s40, s70\n"
    "v_cvt_pk_bf16_f32 v147, v150, v151\n"
    "v_mfma_f32_16x16x32_bf16 v[124:127], v[188:191], v[84:87], v[124:127]\n"
    "s_waitcnt lgkmcnt(5)\n"
    "v_add_u32 v8, s44, v8\n"
    "v_mfma_f32_16x16x32_bf16 v[96:99], v[192:195], v[40:43], v[96:99]\n"
    "v_exp_f32 v152, v152\n"
    "v_mfma_f32_16x16x32_bf16 v[104:107], v[192:195], v[56:59], v[104:107]\n"
    "v_exp_f32 v153, v153\n"
    "v_mfma_f32_16x16x32_bf16 v[112:115], v[192:195], v[72:75], v[112:115]\n"
    "buffer_load_dwordx4 v16, s[24:27], s33 offen lds\n"
    "v_mfma_f32_16x16x32_bf16 v[120:123], v[192:195], v[88:91], v[120:123]\n"
    "s_waitcnt lgkmcnt(4)\n"
    "v_add_u32 v9, s44, v9\n"
    "v_mfma_f32_16x16x32_bf16 v[100:103], v[196:199], v[40:43], v[100:103]\n"
    "v_exp_f32 v154, v154\n"
    "v_mfma_f32_16x16x32_bf16 v[108:111], v[196:199], v[56:59], v[108:111]\n"
    "ds_read_b64_tr_b16 v[216:217], v5 offset:24896\n"
    "v_cvt_pk_bf16_f32 v152, v152, v153\n"
    "v_mfma_f32_16x16x32_bf16 v[116:119], v[196:199], v[72:75], v[116:119]\n"
    "v_exp_f32 v155, v155\n"
    "v_mfma_f32_16x16x32_bf16 v[124:127], v[196:199], v[88:91], v[124:127]\n"
    "s_waitcnt lgkmcnt(3)\n"
    "v_exp_f32 v156, v156\n"
    "v_mfma_f32_16x16x32_bf16 v[96:99], v[200:203], v[44:47], v[96:99]\n"
    "s_add_u32 m0, s40, s71\n"
    "v_cvt_pk_bf16_f32 v153, v154, v155\n"
    "v_mfma_f32_16x16x32_bf16 v[104:107], v[200:203], v[60:63], v[104:107]\n"
    "v_exp_f32 v157, v157\n"
    "v_mfma_f32_16x16x32_bf16 v[112:115], v[200:203], v[76:79], v[112:115]\n"
    "v_exp_f32 v158, v158\n"
    "v_mfma_f32_16x16x32_bf16 v[120:123], v[200:203], v[92:95], v[120:123]\n"
    "s_waitcnt lgkmcnt(1)\n"
    "buffer_load_dwordx4 v17, s[24:27], s33 offen lds\n"
    "v_cvt_pk_bf16_f32 v154, v156, v157\n"
    "v_mfma_f32_16x16x32_bf16 v[100:103], v[204:207], v[44:47], v[100:103]\n"
    "v_exp_f32 v159, v159\n"
    "v_mfma_f32_16x16x32_bf16 v[108:111], v[204:207], v[60:63], v[108:111]\n"
    "ds_read_b64_tr_b16 v[218:219], v5 offset:25408\n"
    "v_cvt_pk_bf16_f32 v155, v158, v159\n"
    "v_mfma_f32_16x16x32_bf16 v[116:119], v[204:207], v[76:79], v[116:119]\n"
    "ds_read_b64_tr_b16 v[220:221], v5 offset:24928\n"
    "ds_read_b64_tr_b16 v[222:223], v5 offset:25440\n"
    "v_mfma_f32_16x16x32_bf16 v[124:127], v[204:207], v[92:95], v[124:127]\n"
    "ds_read_b64_tr_b16 v[224:225], v5 offset:29056\n"
    "ds_read_b64_tr_b16 v[226:227], v5 offset:29568\n"
    "v_mfma_f32_16x16x32_bf16 a[0:3], v[208:211], v[128:131], a[0:3]\n"
    "s_waitcnt vmcnt(10)\n"
    "s_barrier\n"
    "v_mfma_f32_16x16x32_bf16 a[32:35], v[208:211], v[136:139], a[32:35]\n"
    "ds_read_b64_tr_b16 v[228:229], v5 offset:29088\n"
    "ds_read_b64_tr_b16 v[230:231], v5 offset:29600\n"
    "v_mfma_f32_16x16x32_bf16 a[64:67], v[208:211], v[144:147], a[64:67]\n"
    "ds_read_b64_tr_b16 v[232:233], v5 offset:29120\n"
    "ds_read_b64_tr_b16 v[234:235], v5 offset:29632\n"
    "v_mfma_f32_16x16x32_bf16 a[96:99], v[208:211], v[152:155], a[96:99]\n"
    "s_waitcnt lgkmcnt(9)\n"
    "ds_read_b64_tr_b16 v[236:237], v5 offset:29152\n"
    "v_mfma_f32_16x16x32_bf16 a[4:7], v[212:215], v[128:131], a[4:7]\n"
    "s_add_u32 s33, s33, s41\n"
    "ds_read_b64_tr_b16 v[238:239], v5 offset:29664\n"
    "v_mfma_f32_16x16x32_bf16 a[36:39], v[212:215], v[136:139], a[36:39]\n"
    "v_exp_f32 v96, v96\n"
    "v_mfma_f32_16x16x32_bf16 a[68:71], v[212:215], v[144:147], a[68:71]\n"
    "v_exp_f32 v97, v97\n"
    "v_mfma_f32_16x16x32_bf16 a[100:103], v[212:215], v[152:155], a[100:103]\n"
    "s_waitcnt lgkmcnt(10)\n"
    "s_add_u32 s40, s40, s51\n"
    "v_mfma_f32_16x16x32_bf16 a[8:11], v[216:219], v[128:131], a[8:11]\n"
    "v_exp_f32 v98, v98\n"
    "v_mfma_f32_16x16x32_bf16 a[40:43], v[216:219], v[136:139], a[40:43]\n"
    "ds_read_b128 v[176:179], v6 offset:0\n"
    "v_cvt_pk_bf16_f32 v96, v96, v97\n"
    "v_mfma_f32_16x16x32_bf16 a[72:75], v[216:219], v[144:147], a[72:75]\n"
    "v_exp_f32 v99, v99\n"
    "v_mfma_f32_16x16x32_bf16 a[104:107], v[216:219], v[152:155], a[104:107]\n"
    "s_waitcnt lgkmcnt(7)\n"
    "v_add_u32 v5, s44, v5\n"
    "v_mfma_f32_16x16x32_bf16 a[12:15], v[220:223], v[128:131], a[12:15]\n"
    "v_exp_f32 v100, v100\n"
    "v_mfma_f32_16x16x32_bf16 a[44:47], v[220:223], v[136:139], a[44:47]\n"
    "s_cmp_eq_u32 s40, 135168\n"
    "s_cselect_b32 s40, 0, s40\n"
    "v_mfma_f32_16x16x32_bf16 a[76:79], v[220:223], v[144:147], a[76:79]\n"
    "ds_read_b128 v[180:183], v6 offset:4096\n"
    "v_cvt_pk_bf16_f32 v97, v98, v99\n"
    "v_mfma_f32_16x16x32_bf16 a[108:111], v[220:223], v[152:155], a[108:111]\n"
    "s_waitcnt lgkmcnt(4)\n"
    "s_add_u32 s43, s43, s44\n"
    "v_mfma_f32_16x16x32_bf16 a[16:19], v[224:227], v[128:131], a[16:19]\n"
    "v_exp_f32 v101, v101\n"
    "v_mfma_f32_16x16x32_bf16 a[48:51], v[224:227], v[136:139], a[48:51]\n"
    "v_exp_f32 v102, v102\n"
    "v_mfma_f32_16x16x32_bf16 a[80:83], v[224:227], v[144:147], a[80:83]\n"
    "ds_read_b128 v[184:187], v7 offset:0\n"
    "v_cvt_pk_bf16_f32 v98, v100, v101\n"
    "v_mfma_f32_16x16x32_bf16 a[112:115], v[224:227], v[152:155], a[112:115]\n"
    "s_waitcnt lgkmcnt(3)\n"
    "v_exp_f32 v103, v103\n"
    "v_mfma_f32_16x16x32_bf16 a[20:23], v[228:231], v[128:131], a[20:23]\n"
    "ds_read_b128 v[188:191], v7 offset:4096\n"
    "v_cvt_pk_bf16_f32 v99, v102, v103\n"
    "v_mfma_f32_16x16x32_bf16 a[52:55], v[228:231], v[136:139], a[52:55]\n"
    "v_exp_f32 v104, v104\n"
    "v_mfma_f32_16x16x32_bf16 a[84:87], v[228:231], v[144:147], a[84:87]\n"
    "v_exp_f32 v105, v105\n"
    "v_mfma_f32_16x16x32_bf16 a[116:119], v[228:231], v[152:155], a[116:119]\n"
    "v_exp_f32 v106, v106\n"
    "v_mfma_f32_16x16x32_bf16 a[24:27], v[232:235], v[128:131], a[24:27]\n"
    "ds_read_b128 v[192:195], v8 offset:0\n"
    "v_cvt_pk_bf16_f32 v104, v104, v105\n"
    "v_mfma_f32_16x16x32_bf16 a[56:59], v[232:235], v[136:139], a[56:59]\n"
    "v_exp_f32 v107, v107\n"
    "v_mfma_f32_16x16x32_bf16 a[88:91], v[232:235], v[144:147], a[88:91]\n"
    "v_exp_f32 v108, v108\n"
    "v_mfma_f32_16x16x32_bf16 a[120:123], v[232:235], v[152:155], a[120:123]\n"
    "ds_read_b128 v[196:199], v8 offset:4096\n"
    "v_cvt_pk_bf16_f32 v105, v106, v107\n"
    "v_mfma_f32_16x16x32_bf16 a[28:31], v[236:239], v[128:131], a[28:31]\n"
    "v_exp_f32 v109, v109\n"
    "v_mfma_f32_16x16x32_bf16 a[60:63], v[236:239], v[136:139], a[60:63]\n"
    "s_cmp_eq_u32 s43, 101376\n"
    "s_cselect_b32 s44, -101376, s51\n"
    "v_mfma_f32_16x16x32_bf16 a[92:95], v[236:239], v[144:147], a[92:95]\n"
    "v_exp_f32 v110, v110\n"
    "v_mfma_f32_16x16x32_bf16 a[124:127], v[236:239], v[152:155], a[124:127]\n"
    "ds_read_b128 v[200:203], v9 offset:0\n"
    "v_cvt_pk_bf16_f32 v106, v108, v109\n"
    "v_mfma_f32_16x16x32_bf16 a[128:131], a[240:243], v[128:131], a[128:131]\n"
    "ds_read_b128 v[204:207], v9 offset:4096\n"
    "v_add_u32 v29, 2048, v29\n"
    "v_mfma_f32_16x16x32_bf16 a[132:135], a[240:243], v[136:139], a[132:135]\n"
    "v_exp_f32 v111, v111\n"
    "v_mfma_f32_16x16x32_bf16 a[136:139], a[240:243], v[144:147], a[136:139]\n"
    "v_cvt_pk_bf16_f32 v107, v110, v111\n"
    "v_mfma_f32_16x16x32_bf16 a[140:143], a[240:243], v[152:155], a[140:143]\n"
    "s_cmp_lg_u32 s50, 2\n"
    "s_cbranch_scc1 K1_A1\n"
    "v_mov_b32 v10, v160\n"
    "v_mov_b32 v11, v161\n"
    "v_mov_b32 v12, v162\n"
    "v_mov_b32 v13, v163\n"
    "v_mov_b32 v14, v160\n"
    "v_mov_b32 v15, v161\n"
    "v_mov_b32 v16, v162\n"
    "v_mov_b32 v17, v163\n"
    "s_mov_b64 s[20:21], s[88:89]\n"
    "s_mov_b64 s[24:25], s[92:93]\n"
    "s_mov_b32 s22, s91\n"
    "s_mov_b32 s26, s91\n"
    "s_mov_b32 s41, 442368\n"
    "s_mov_b32 s33, 0\n"
    "s_add_u32 s64, s59, 0\n"
    "s_add_u32 s65, s59, 8448\n"
    "s_add_u32 s66, s59, 16896\n"
    "s_add_u32 s67, s59, 25344\n"
    "s_add_u32 s68, s59, 4224\n"
    "s_add_u32 s69, s59, 12672\n"
    "s_add_u32 s70, s59, 21120\n"
    "s_add_u32 s71, s59, 29568\n"
    "K1_A1:\n"
    "s_cmp_lg_u32 s50, 0\n"
    "s_cbranch_scc1 K1_AT\n"
    "s_lshl_b32 s56, s74, 12\n"
    "s_mov_b32 s58, 0\n"
    "s_mov_b32 s52, 0x21800000\n"
    "s_mov_b32 s53, 0x5d800000\n"
    "v_accvgpr_read_b32 v20, a128\n"
    "v_accvgpr_read_b32 v21, a132\n"
    "v_accvgpr_read_b32 v22, a136\n"
    "v_accvgpr_read_b32 v23, a140\n"
    "v_add_u32 v3, s43, v18\n"
    "v_rcp_f32 v224, v20\n"
    "v_rcp_f32 v226, v21\n"
    "v_rcp_f32 v228, v22\n"
    "v_rcp_f32 v230, v23\n"
    "v_min3_f32 v24, v20, v21, v22\n"
    "v_min_f32 v24, v24, v23\n"
    "v_max3_f32 v25, v20, v21, v22\n"
    "v_max_f32 v25, v25, v23\n"
    "v_add_f32 v26, v20, v21\n"
    "v_add_f32 v27, v22, v23\n"
    "v_add_f32 v26, v26, v27\n"
    "v_cmp_nlt_f32 vcc, s52, v24\n"
    "s_nop 0\n"
    "s_mov_b64 s[94:95], vcc\n"
    "v_cmp_ngt_f32 vcc, s53, v25\n"
    "s_nop 0\n"
    "s_or_b64 s[94:95], s[94:95], vcc\n"
    "v_cmp_u_f32 vcc, v26, v26\n"
    "s_nop 0\n"
    "s_or_b64 s[94:95], s[94:95], vcc\n"
    "s_or_b32 s58, s94, s95\n"
    "s_add_u32 s52, s56, 0\n"
    "s_add_u32 s53, s56, 188416\n"
    "s_add_u32 s54, s56, 376832\n"
    "s_add_u32 s55, s56, 565248\n"
    "s_cmp_ge_u32 s74, 27\n"
    "s_cbranch_scc1 K1_EX\n"
    "ds_read_b64_tr_b16 a[208:209], v3 offset:0\n"
    "ds_read_b64_tr_b16 a[210:211], v3 offset:512\n"
    "ds_read_b64_tr_b16 a[212:213], v3 offset:32\n"
    "ds_read_b64_tr_b16 a[214:215], v3 offset:544\n"
    "ds_read_b64_tr_b16 a[216:217], v3 offset:64\n"
    "ds_read_b64_tr_b16 a[218:219], v3 offset:576\n"
    "ds_read_b64_tr_b16 a[220:221], v3 offset:96\n"
    "ds_read_b64_tr_b16 a[222:223], v3 offset:608\n"
    "ds_read_b64_tr_b16 a[224:225], v3 offset:4224\n"
    "ds_read_b64_tr_b16 a[226:227], v3 offset:4736\n"
    "ds_read_b64_tr_b16 a[228:229], v3 offset:4256\n"
    "ds_read_b64_tr_b16 a[230:231], v3 offset:4768\n"
    "s_waitcnt lgkmcnt(10)\n"
    "v_mfma_f32_16x16x32_bf16 v[32:35], a[208:211], a[144:147], 0\n"
    "ds_read_b64_tr_b16 a[232:233], v3 offset:4288\n"
    "v_mfma_f32_16x16x32_bf16 v[64:67], a[208:211], a[148:151], 0\n"
    "ds_read_b64_tr_b16 a[234:235], v3 offset:4800\n"
    "ds_read_b64_tr_b16 a[236:237], v3 offset:4320\n"
    "v_mfma_f32_16x16x32_bf16 v[96:99], a[208:211], a[152:155], 0\n"
    "ds_read_b64_tr_b16 a[238:239], v3 offset:4832\n"
    "buffer_load_dwordx4 a[192:195], v19, s[28:31], 0 offen offset:3072\n"
    "v_mfma_f32_16x16x32_bf16 v[128:131], a[208:211], a[156:159], 0\n"
    "s_waitcnt lgkmcnt(12)\n"
    "v_accvgpr_read_b32 v176, a0\n"
    "v_mfma_f32_16x16x32_bf16 v[36:39], a[212:215], a[144:147], 0\n"
    "v_accvgpr_read_b32 v177, a1\n"
    "v_accvgpr_read_b32 v178, a2\n"
    "v_mfma_f32_16x16x32_bf16 v[68:71], a[212:215], a[148:151], 0\n"
    "s_add_u32 m0, s40, s64\n"
    "ds_read_b64_tr_b16 a[208:209], v3 offset:8448\n"
    "v_mfma_f32_16x16x32_bf16 v[100:103], a[212:215], a[152:155], 0\n"
    "ds_read_b64_tr_b16 a[210:211], v3 offset:8960\n"
    "v_accvgpr_read_b32 v179, a3\n"
    "v_mfma_f32_16x16x32_bf16 v[132:135], a[212:215], a[156:159], 0\n"
    "s_waitcnt lgkmcnt(12)\n"
    "v_accvgpr_read_b32 v180, a4\n"
    "v_mfma_f32_16x16x32_bf16 v[40:43], a[216:219], a[144:147], 0\n"
    "buffer_load_dwordx4 v10, s[20:23], s33 offen lds\n"
    "v_mfma_f32_16x16x32_bf16 v[72:75], a[216:219], a[148:151], 0\n"
    "ds_read_b64_tr_b16 a[212:213], v3 offset:8480\n"
    "ds_read_b64_tr_b16 a[214:215], v3 offset:8992\n"
    "v_mfma_f32_16x16x32_bf16 v[104:107], a[216:219], a[152:155], 0\n"
    "buffer_load_dwordx4 a[196:199], v19, s[28:31], s46 offen offset:3072\n"
    "v_accvgpr_read_b32 v181, a5\n"
    "v_mfma_f32_16x16x32_bf16 v[136:139], a[216:219], a[156:159], 0\n"
    "s_waitcnt lgkmcnt(8)\n"
    "v_accvgpr_read_b32 v182, a6\n"
    "v_mfma_f32_16x16x32_bf16 v[44:47], a[220:223], a[144:147], 0\n"
    "v_accvgpr_read_b32 v183, a7\n"
    "v_mul_f32 v176, v176, v224\n"
    "v_mfma_f32_16x16x32_bf16 v[76:79], a[220:223], a[148:151], 0\n"
    "ds_read_b64_tr_b16 a[216:217], v3 offset:8512\n"
    "ds_read_b64_tr_b16 a[218:219], v3 offset:9024\n"
    "v_mfma_f32_16x16x32_bf16 v[108:111], a[220:223], a[152:155], 0\n"
    "v_mul_f32 v177, v177, v224\n"
    "v_mul_f32 v178, v178, v224\n"
    "v_mfma_f32_16x16x32_bf16 v[140:143], a[220:223], a[156:159], 0\n"
    "s_waitcnt lgkmcnt(8)\n"
    "v_mul_f32 v179, v179, v224\n"
    "v_mfma_f32_16x16x32_bf16 v[48:51], a[224:227], a[144:147], 0\n"
    "v_mul_f32 v180, v180, v224\n"
    "v_mul_f32 v181, v181, v224\n"
    "v_mfma_f32_16x16x32_bf16 v[80:83], a[224:227], a[148:151], 0\n"
    "ds_read_b64_tr_b16 a[220:221], v3 offset:8544\n"
    "ds_read_b64_tr_b16 a[222:223], v3 offset:9056\n"
    "v_mfma_f32_16x16x32_bf16 v[112:115], a[224:227], a[152:155], 0\n"
    "s_add_u32 m0, s40, s65\n"
    "buffer_load_dwordx4 a[200:203], v19, s[28:31], s47 offen offset:3072\n"
    "v_mfma_f32_16x16x32_bf16 v[144:147], a[224:227], a[156:159], 0\n"
    "s_waitcnt lgkmcnt(8)\n"
    "v_mul_f32 v182, v182, v224\n"
    "v_mfma_f32_16x16x32_bf16 v[52:55], a[228:231], a[144:147], 0\n"
    "buffer_load_dwordx4 v11, s[20:23], s33 offen lds\n"
    "v_mfma_f32_16x16x32_bf16 v[84:87], a[228:231], a[148:151], 0\n"
    "ds_read_b64_tr_b16 a[224:225], v3 offset:12672\n"
    "ds_read_b64_tr_b16 a[226:227], v3 offset:13184\n"
    "v_mfma_f32_16x16x32_bf16 v[116:119], a[228:231], a[152:155], 0\n"
    "v_mul_f32 v183, v183, v224\n"
    "v_cvt_pk_bf16_f32 v184, v176, v177\n"
    "v_mfma_f32_16x16x32_bf16 v[148:151], a[228:231], a[156:159], 0\n"
    "s_waitcnt lgkmcnt(8)\n"
    "v_cvt_pk_bf16_f32 v185, v178, v179\n"
    "v_mfma_f32_16x16x32_bf16 v[56:59], a[232:235], a[144:147], 0\n"
    "v_cvt_pk_bf16_f32 v186, v180, v181\n"
    "v_cvt_pk_bf16_f32 v187, v182, v183\n"
    "v_mfma_f32_16x16x32_bf16 v[88:91], a[232:235], a[148:151], 0\n"
    "ds_read_b64_tr_b16 a[228:229], v3 offset:12704\n"
    "ds_read_b64_tr_b16 a[230:231], v3 offset:13216\n"
    "v_mfma_f32_16x16x32_bf16 v[120:123], a[232:235], a[152:155], 0\n"
    "buffer_load_dwordx4 a[204:207], v19, s[28:31], s48 offen offset:3072\n"
    "buffer_store_dwordx4 v[184:187], v2, s[36:39], s52 offen offset:0\n"
    "v_mfma_f32_16x16x32_bf16 v[152:155], a[232:235], a[156:159], 0\n"
    "s_waitcnt lgkmcnt(8)\n"
    "v_accvgpr_read_b32 v188, a8\n"
    "v_mfma_f32_16x16x32_bf16 v[60:63], a[236:239], a[144:147], 0\n"
    "v_accvgpr_read_b32 v189, a9\n"
    "v_accvgpr_read_b32 v190, a10\n"
    "v_mfma_f32_16x16x32_bf16 v[92:95], a[236:239], a[148:151], 0\n"
    "ds_read_b64_tr_b16 a[232:233], v3 offset:12736\n"
    "ds_read_b64_tr_b16 a[234:235], v3 offset:13248\n"
    "v_mfma_f32_16x16x32_bf16 v[124:127], a[236:239], a[152:155], 0\n"
    "v_mov_b32 v19, v4\n"
    "v_accvgpr_read_b32 v191, a11\n"
    "v_mfma_f32_16x16x32_bf16 v[156:159], a[236:239], a[156:159], 0\n"
    "s_waitcnt lgkmcnt(8)\n"
    "s_add_u32 m0, s40, s66\n"
    "v_mfma_f32_16x16x32_bf16 v[32:35], a[208:211], a[160:163], v[32:35]\n"
    "buffer_load_dwordx4 v12, s[20:23], s33 offen lds\n"
    "v_mfma_f32_16x16x32_bf16 v[64:67], a[208:211], a[164:167], v[64:67]\n"
    "ds_read_b64_tr_b16 a[236:237], v3 offset:12768\n"
    "ds_read_b64_tr_b16 a[238:239], v3 offset:13280\n"
    "v_mfma_f32_16x16x32_bf16 v[96:99], a[208:211], a[168:171], v[96:99]\n"
    "buffer_load_dwordx4 a[144:147], v4, s[28:31], 0 offen offset:0\n"
    "v_accvgpr_read_b32 v192, a12\n"
    "v_mfma_f32_16x16x32_bf16 v[128:131], a[208:211], a[172:175], v[128:131]\n"
    "s_waitcnt lgkmcnt(8)\n"
    "v_accvgpr_read_b32 v193, a13\n"
    "v_mfma_f32_16x16x32_bf16 v[36:39], a[212:215], a[160:163], v[36:39]\n"
    "v_accvgpr_read_b32 v194, a14\n"
    "v_accvgpr_read_b32 v195, a15\n"
    "v_mfma_f32_16x16x32_bf16 v[68:71], a[212:215], a[164:167], v[68:71]\n"
    "ds_read_b64_tr_b16 a[208:209], v3 offset:16896\n"
    "ds_read_b64_tr_b16 a[210:211], v3 offset:17408\n"
    "v_mfma_f32_16x16x32_bf16 v[100:103], a[212:215], a[168:171], v[100:103]\n"
    "v_mul_f32 v188, v188, v224\n"
    "v_mul_f32 v189, v189, v224\n"
    "v_mfma_f32_16x16x32_bf16 v[132:135], a[212:215], a[172:175], v[132:135]\n"
    "s_waitcnt lgkmcnt(8)\n"
    "v_mul_f32 v190, v190, v224\n"
    "v_mfma_f32_16x16x32_bf16 v[40:43], a[216:219], a[160:163], v[40:43]\n"
    "v_mul_f32 v191, v191, v224\n"
    "v_mul_f32 v192, v192, v224\n"
    "v_mfma_f32_16x16x32_bf16 v[72:75], a[216:219], a[164:167], v[72:75]\n"
    "ds_read_b64_tr_b16 a[212:213], v3 offset:16928\n"
    "ds_read_b64_tr_b16 a[214:215], v3 offset:17440\n"
    "v_mfma_f32_16x16x32_bf16 v[104:107], a[216:219], a[168:171], v[104:107]\n"
    "buffer_load_dwordx4 a[148:151], v4, s[28:31], s46 offen offset:0\n"
    "v_mul_f32 v193, v193, v224\n"
    "v_mfma_f32_16x16x32_bf16 v[136:139], a[216:219], a[172:175], v[136:139]\n"
    "s_waitcnt lgkmcnt(8)\n"
    "v_mul_f32 v194, v194, v224\n"
    "v_mfma_f32_16x16x32_bf16 v[44:47], a[220:223], a[160:163], v[44:47]\n"
    "s_add_u32 m0, s40, s67\n"
    "v_mul_f32 v195, v195, v224\n"
    "v_mfma_f32_16x16x32_bf16 v[76:79], a[220:223], a[164:167], v[76:79]\n"
    "ds_read_b64_tr_b16 a[216:217], v3 offset:16960\n"
    "ds_read_b64_tr_b16 a[218:219], v3 offset:17472\n"
    "v_mfma_f32_16x16x32_bf16 v[108:111], a[220:223], a[168:171], v[108:111]\n"
    "buffer_load_dwordx4 v13, s[20:23], s33 offen lds\n"
    "v_mfma_f32_16x16x32_bf16 v[140:143], a[220:223], a[172:175], v[140:143]\n"
    "s_waitcnt lgkmcnt(8)\n"
    "v_cvt_pk_bf16_f32 v196, v188, v189\n"
    "v_mfma_f32_16x16x32_bf16 v[48:51], a[224:227], a[160:163], v[48:51]\n"
    "v_cvt_pk_bf16_f32 v197, v190, v191\n"
    "v_cvt_pk_bf16_f32 v198, v192, v193\n"
    "v_mfma_f32_16x16x32_bf16 v[80:83], a[224:227], a[164:167], v[80:83]\n"
    "ds_read_b64_tr_b16 a[220:221], v3 offset:16992\n"
    "ds_read_b64_tr_b16 a[222:223], v3 offset:17504\n"
    "v_mfma_f32_16x16x32_bf16 v[112:115], a[224:227], a[168:171], v[112:115]\n"
    "buffer_load_dwordx4 a[152:155], v4, s[28:31], s47 offen offset:0\n"
    "v_cvt_pk_bf16_f32 v199, v194, v195\n"
    "v_mfma_f32_16x16x32_bf16 v[144:147], a[224:227], a[172:175], v[144:147]\n"
    "s_waitcnt lgkmcnt(8)\n"
    "buffer_store_dwordx4 v[196:199], v2, s[36:39], s52 offen offset:1024\n"
    "v_mfma_f32_16x16x32_bf16 v[52:55], a[228:231], a[160:163], v[52:55]\n"
    "v_accvgpr_read_b32 v200, a16\n"
    "v_accvgpr_read_b32 v201, a17\n"
    "v_mfma_f32_16x16x32_bf16 v[84:87], a[228:231], a[164:167], v[84:87]\n"
    "ds_read_b64_tr_b16 a[224:225], v3 offset:21120\n"
    "ds_read_b64_tr_b16 a[226:227], v3 offset:21632\n"
    "v_mfma_f32_16x16x32_bf16 v[116:119], a[228:231], a[168:171], v[116:119]\n"
    "s_sub_u32 s50, s50, 1\n"
    "v_accvgpr_read_b32 v202, a18\n"
    "v_mfma_f32_16x16x32_bf16 v[148:151], a[228:231], a[172:175], v[148:151]\n"
    "s_waitcnt lgkmcnt(8)\n"
    "v_accvgpr_read_b32 v203, a19\n"
    "v_mfma_f32_16x16x32_bf16 v[56:59], a[232:235], a[160:163], v[56:59]\n"
    "v_accvgpr_read_b32 v204, a20\n"
    "v_accvgpr_read_b32 v205, a21\n"
    "v_mfma_f32_16x16x32_bf16 v[88:91], a[232:235], a[164:167], v[88:91]\n"
    "s_add_u32 m0, s40, s68\n"
    "ds_read_b64_tr_b16 a[228:229], v3 offset:21152\n"
    "v_mfma_f32_16x16x32_bf16 v[120:123], a[232:235], a[168:171], v[120:123]\n"
    "ds_read_b64_tr_b16 a[230:231], v3 offset:21664\n"
    "buffer_load_dwordx4 a[156:159], v4, s[28:31], s48 offen offset:0\n"
    "v_mfma_f32_16x16x32_bf16 v[152:155], a[232:235], a[172:175], v[152:155]\n"
    "s_waitcnt lgkmcnt(8)\n"
    "v_accvgpr_read_b32 v206, a22\n"
    "v_mfma_f32_16x16x32_bf16 v[60:63], a[236:239], a[160:163], v[60:63]\n"
    "buffer_load_dwordx4 v14, s[24:27], s33 offen lds\n"
    "v_mfma_f32_16x16x32_bf16 v[92:95], a[236:239], a[164:167], v[92:95]\n"
    "ds_read_b64_tr_b16 a[232:233], v3 offset:21184\n"
    "ds_read_b64_tr_b16 a[234:235], v3 offset:21696\n"
    "v_mfma_f32_16x16x32_bf16 v[124:127], a[236:239], a[168:171], v[124:127]\n"
    "v_accvgpr_read_b32 v207, a23\n"
    "v_mul_f32 v200, v200, v224\n"
    "v_mfma_f32_16x16x32_bf16 v[156:159], a[236:239], a[172:175], v[156:159]\n"
    "s_waitcnt lgkmcnt(8)\n"
    "v_mul_f32 v201, v201, v224\n"
    "v_mfma_f32_16x16x32_bf16 v[32:35], a[208:211], a[176:179], v[32:35]\n"
    "v_mul_f32 v202, v202, v224\n"
    "v_mul_f32 v203, v203, v224\n"
    "v_mfma_f32_16x16x32_bf16 v[64:67], a[208:211], a[180:183], v[64:67]\n"
    "ds_read_b64_tr_b16 a[236:237], v3 offset:21216\n"
    "ds_read_b64_tr_b16 a[238:239], v3 offset:21728\n"
    "v_mfma_f32_16x16x32_bf16 v[96:99], a[208:211], a[184:187], v[96:99]\n"
    "s_add_u32 s49, s49, 4096\n"
    "buffer_load_dwordx4 a[160:163], v4, s[28:31], 0 offen offset:1024\n"
    "v_mfma_f32_16x16x32_bf16 v[128:131], a[208:211], a[188:191], v[128:131]\n"
    "s_waitcnt lgkmcnt(8)\n"
    "v_mul_f32 v204, v204, v224\n"
    "v_mfma_f32_16x16x32_bf16 v[36:39], a[212:215], a[176:179], v[36:39]\n"
    "v_mul_f32 v205, v205, v224\n"
    "v_mul_f32 v206, v206, v224\n"
    "v_mfma_f32_16x16x32_bf16 v[68:71], a[212:215], a[180:183], v[68:71]\n"
    "ds_read_b64_tr_b16 a[208:209], v3 offset:25344\n"
    "ds_read_b64_tr_b16 a[210:211], v3 offset:25856\n"
    "v_mfma_f32_16x16x32_bf16 v[100:103], a[212:215], a[184:187], v[100:103]\n"
    "s_add_u32 m0, s40, s69\n"
    "v_mul_f32 v207, v207, v224\n"
    "v_mfma_f32_16x16x32_bf16 v[132:135], a[212:215], a[188:191], v[132:135]\n"
    "s_waitcnt lgkmcnt(8)\n"
    "v_cvt_pk_bf16_f32 v208, v200, v201\n"
    "v_mfma_f32_16x16x32_bf16 v[40:43], a[216:219], a[176:179], v[40:43]\n"
    "buffer_load_dwordx4 v15, s[24:27], s33 offen lds\n"
    "v_mfma_f32_16x16x32_bf16 v[72:75], a[216:219], a[180:183], v[72:75]\n"
    "ds_read_b64_tr_b16 a[212:213], v3 offset:25376\n"
    "v_cvt_pk_bf16_f32 v209, v202, v203\n"
    "v_mfma_f32_16x16x32_bf16 v[104:107], a[216:219], a[184:187], v[104:107]\n"
    "ds_read_b64_tr_b16 a[214:215], v3 offset:25888\n"
    "buffer_load_dwordx4 a[164:167], v4, s[28:31], s46 offen offset:1024\n"
    "v_mfma_f32_16x16x32_bf16 v[136:139], a[216:219], a[188:191], v[136:139]\n"
    "s_waitcnt lgkmcnt(8)\n"
    "v_cvt_pk_bf16_f32 v210, v204, v205\n"
    "v_mfma_f32_16x16x32_bf16 v[44:47], a[220:223], a[176:179], v[44:47]\n"
    "v_cvt_pk_bf16_f32 v211, v206, v207\n"
    "buffer_store_dwordx4 v[208:211], v2, s[36:39], s52 offen offset:2048\n"
    "v_mfma_f32_16x16x32_bf16 v[76:79], a[220:223], a[180:183], v[76:79]\n"
    "ds_read_b64_tr_b16 a[216:217], v3 offset:25408\n"
    "v_accvgpr_read_b32 v212, a24\n"
    "v_mfma_f32_16x16x32_bf16 v[108:111], a[220:223], a[184:187], v[108:111]\n"
    "s_cmp_eq_u32 s49, 20480\n"
    "s_cselect_b32 s49, 0, s49\n"
    "v_mfma_f32_16x16x32_bf16 v[140:143], a[220:223], a[188:191], v[140:143]\n"
    "s_waitcnt lgkmcnt(7)\n"
    "ds_read_b64_tr_b16 a[218:219], v3 offset:25920\n"
    "v_mfma_f32_16x16x32_bf16 v[48:51], a[224:227], a[176:179], v[48:51]\n"
    "v_accvgpr_read_b32 v213, a25\n"
    "v_accvgpr_read_b32 v214, a26\n"
    "v_mfma_f32_16x16x32_bf16 v[80:83], a[224:227], a[180:183], v[80:83]\n"
    "ds_read_b64_tr_b16 a[220:221], v3 offset:25440\n"
    "v_accvgpr_read_b32 v215, a27\n"
    "v_mfma_f32_16x16x32_bf16 v[112:115], a[224:227], a[184:187], v[112:115]\n"
    "ds_read_b64_tr_b16 a[222:223], v3 offset:25952\n"
    "buffer_load_dwordx4 a[168:171], v4, s[28:31], s47 offen offset:1024\n"
    "v_mfma_f32_16x16x32_bf16 v[144:147], a[224:227], a[188:191], v[144:147]\n"
    "s_waitcnt lgkmcnt(8)\n"
    "v_accvgpr_read_b32 v216, a28\n"
    "v_mfma_f32_16x16x32_bf16 v[52:55], a[228:231], a[176:179], v[52:55]\n"
    "s_add_u32 m0, s40, s70\n"
    "v_accvgpr_read_b32 v217, a29\n"
    "v_mfma_f32_16x16x32_bf16 v[84:87], a[228:231], a[180:183], v[84:87]\n"
    "ds_read_b64_tr_b16 a[224:225], v3 offset:29568\n"
    "v_accvgpr_read_b32 v218, a30\n"
    "v_mfma_f32_16x16x32_bf16 v[116:119], a[228:231], a[184:187], v[116:119]\n"
    "buffer_load_dwordx4 v16, s[24:27], s33 offen lds\n"
    "v_mfma_f32_16x16x32_bf16 v[148:151], a[228:231], a[188:191], v[148:151]\n"
    "s_waitcnt lgkmcnt(7)\n"
    "v_accvgpr_read_b32 v219, a31\n"
    "v_mfma_f32_16x16x32_bf16 v[56:59], a[232:235], a[176:179], v[56:59]\n"
    "ds_read_b64_tr_b16 a[226:227], v3 offset:30080\n"
    "v_mul_f32 v212, v212, v224\n"
    "v_mfma_f32_16x16x32_bf16 v[88:91], a[232:235], a[180:183], v[88:91]\n"
    "v_mul_f32 v213, v213, v224\n"
    "v_mul_f32 v214, v214, v224\n"
    "v_mfma_f32_16x16x32_bf16 v[120:123], a[232:235], a[184:187], v[120:123]\n"
    "ds_read_b64_tr_b16 a[228:229], v3 offset:29600\n"
    "v_mul_f32 v215, v215, v224\n"
    "v_mfma_f32_16x16x32_bf16 v[152:155], a[232:235], a[188:191], v[152:155]\n"
    "ds_read_b64_tr_b16 a[230:231], v3 offset:30112\n"
    "buffer_load_dwordx4 a[172:175], v4, s[28:31], s48 offen offset:1024\n"
    "v_mfma_f32_16x16x32_bf16 v[60:63], a[236:239], a[176:179], v[60:63]\n"
    "v_mul_f32 v216, v216, v224\n"
    "v_mul_f32 v217, v217, v224\n"
    "v_mfma_f32_16x16x32_bf16 v[92:95], a[236:239], a[180:183], v[92:95]\n"
    "ds_read_b64_tr_b16 a[232:233], v3 offset:29632\n"
    "v_mul_f32 v218, v218, v224\n"
    "v_mfma_f32_16x16x32_bf16 v[124:127], a[236:239], a[184:187], v[124:127]\n"
    "ds_read_b64_tr_b16 a[234:235], v3 offset:30144\n"
    "v_mul_f32 v219, v219, v224\n"
    "v_mfma_f32_16x16x32_bf16 v[156:159], a[236:239], a[188:191], v[156:159]\n"
    "s_waitcnt vmcnt(16) lgkmcnt(8)\n"
    "v_cvt_pk_bf16_f32 v220, v212, v213\n"
    "v_mfma_f32_16x16x32_bf16 v[32:35], a[208:211], a[192:195], v[32:35]\n"
    "s_add_u32 m0, s40, s71\n"
    "v_cvt_pk_bf16_f32 v221, v214, v215\n"
    "v_mfma_f32_16x16x32_bf16 v[64:67], a[208:211], a[196:199], v[64:67]\n"
    "ds_read_b64_tr_b16 a[236:237], v3 offset:29664\n"
    "v_cvt_pk_bf16_f32 v222, v216, v217\n"
    "v_mfma_f32_16x16x32_bf16 v[96:99], a[208:211], a[200:203], v[96:99]\n"
    "ds_read_b64_tr_b16 a[238:239], v3 offset:30176\n"
    "buffer_load_dwordx4 a[176:179], v4, s[28:31], 0 offen offset:2048\n"
    "v_mfma_f32_16x16x32_bf16 v[128:131], a[208:211], a[204:207], v[128:131]\n"
    "s_barrier\n"
    "v_cvt_pk_bf16_f32 v223, v218, v219\n"
    "v_mfma_f32_16x16x32_bf16 v[36:39], a[212:215], a[192:195], v[36:39]\n"
    "buffer_load_dwordx4 v17, s[24:27], s33 offen lds\n"
    "v_mfma_f32_16x16x32_bf16 v[68:71], a[212:215], a[196:199], v[68:71]\n"
    "buffer_store_dwordx4 v[220:223], v2, s[36:39], s52 offen offset:3072\n"
    "v_add_u32 v3, s44, v3\n"
    "v_mfma_f32_16x16x32_bf16 v[100:103], a[212:215], a[200:203], v[100:103]\n"
    "v_accvgpr_read_b32 v176, a32\n"
    "v_accvgpr_read_b32 v177, a33\n"
    "v_mfma_f32_16x16x32_bf16 v[132:135], a[212:215], a[204:207], v[132:135]\n"
    "s_waitcnt lgkmcnt(6)\n"
    "ds_read_b64_tr_b16 a[208:209], v3 offset:0\n"
    "v_mfma_f32_16x16x32_bf16 v[40:43], a[216:219], a[192:195], v[40:43]\n"
    "ds_read_b64_tr_b16 a[210:211], v3 offset:512\n"
    "v_accvgpr_read_b32 v178, a34\n"
    "v_mfma_f32_16x16x32_bf16 v[72:75], a[216:219], a[196:199], v[72:75]\n"
    "buffer_load_dwordx4 a[180:183], v4, s[28:31], s46 offen offset:2048\n"
    "v_accvgpr_read_b32 v179, a35\n"
    "v_mfma_f32_16x16x32_bf16 v[104:107], a[216:219], a[200:203], v[104:107]\n"
    "ds_read_b64_tr_b16 a[212:213], v3 offset:32\n"
    "v_accvgpr_read_b32 v180, a36\n"
    "v_mfma_f32_16x16x32_bf16 v[136:139], a[216:219], a[204:207], v[136:139]\n"
    "s_waitcnt lgkmcnt(7)\n"
    "ds_read_b64_tr_b16 a[214:215], v3 offset:544\n"
    "v_mfma_f32_16x16x32_bf16 v[44:47], a[220:223], a[192:195], v[44:47]\n"
    "v_accvgpr_read_b32 v181, a37\n"
    "v_accvgpr_read_b32 v182, a38\n"
    "v_mfma_f32_16x16x32_bf16 v[76:79], a[220:223], a[196:199], v[76:79]\n"
    "ds_read_b64_tr_b16 a[216:217], v3 offset:64\n"
    "v_accvgpr_read_b32 v183, a39\n"
    "v_mfma_f32_16x16x32_bf16 v[108:111], a[220:223], a[200:203], v[108:111]\n"
    "ds_read_b64_tr_b16 a[218:219], v3 offset:576\n"
    "v_mul_f32 v176, v176, v226\n"
    "v_mfma_f32_16x16x32_bf16 v[140:143], a[220:223], a[204:207], v[140:143]\n"
    "s_waitcnt lgkmcnt(8)\n"
    "buffer_load_dwordx4 a[184:187], v4, s[28:31], s47 offen offset:2048\n"
    "v_mfma_f32_16x16x32_bf16 v[48:51], a[224:227], a[192:195], v[48:51]\n"
    "v_mul_f32 v177, v177, v226\n"
    "v_mul_f32 v178, v178, v226\n"
    "v_mfma_f32_16x16x32_bf16 v[80:83], a[224:227], a[196:199], v[80:83]\n"
    "ds_read_b64_tr_b16 a[220:221], v3 offset:96\n"
    "v_mul_f32 v179, v179, v226\n"
    "v_mfma_f32_16x16x32_bf16 v[112:115], a[224:227], a[200:203], v[112:115]\n"
    "ds_read_b64_tr_b16 a[222:223], v3 offset:608\n"
    "v_mul_f32 v180, v180, v226\n"
    "v_mfma_f32_16x16x32_bf16 v[144:147], a[224:227], a[204:207], v[144:147]\n"
    "s_waitcnt lgkmcnt(8)\n"
    "v_mul_f32 v181, v181, v226\n"
    "v_mfma_f32_16x16x32_bf16 v[52:55], a[228:231], a[192:195], v[52:55]\n"
    "buffer_load_dwordx4 a[188:191], v4, s[28:31], s48 offen offset:2048\n"
    "v_mul_f32 v182, v182, v226\n"
    "v_mfma_f32_16x16x32_bf16 v[84:87], a[228:231], a[196:199], v[84:87]\n"
    "s_add_u32 s43, s43, s44\n"
    "ds_read_b64_tr_b16 a[224:225], v3 offset:4224\n"
    "v_mfma_f32_16x16x32_bf16 v[116:119], a[228:231], a[200:203], v[116:119]\n"
    "ds_read_b64_tr_b16 a[226:227], v3 offset:4736\n"
    "v_mul_f32 v183, v183, v226\n"
    "v_mfma_f32_16x16x32_bf16 v[148:151], a[228:231], a[204:207], v[148:151]\n"
    "s_waitcnt lgkmcnt(8)\n"
    "s_add_u32 s33, s33, s41\n"
    "v_mfma_f32_16x16x32_bf16 v[56:59], a[232:235], a[192:195], v[56:59]\n"
    "v_cvt_pk_bf16_f32 v184, v176, v177\n"
    "v_cvt_pk_bf16_f32 v185, v178, v179\n"
    "v_mfma_f32_16x16x32_bf16 v[88:91], a[232:235], a[196:199], v[88:91]\n"
    "s_cmp_eq_u32 s43, 101376\n"
    "s_cselect_b32 s44, -101376, s51\n"
    "v_mfma_f32_16x16x32_bf16 v[120:123], a[232:235], a[200:203], v[120:123]\n"
    "s_add_u32 s40, s40, s51\n"
    "v_cvt_pk_bf16_f32 v186, v180, v181\n"
    "v_mfma_f32_16x16x32_bf16 v[152:155], a[232:235], a[204:207], v[152:155]\n"
    "s_waitcnt lgkmcnt(6)\n"
    "ds_read_b64_tr_b16 a[228:229], v3 offset:4256\n"
    "v_mfma_f32_16x16x32_bf16 v[60:63], a[236:239], a[192:195], v[60:63]\n"
    "ds_read_b64_tr_b16 a[230:231], v3 offset:4768\n"
    "v_cvt_pk_bf16_f32 v187, v182, v183\n"
    "v_mfma_f32_16x16x32_bf16 v[92:95], a[236:239], a[196:199], v[92:95]\n"
    "s_cmp_eq_u32 s40, 135168\n"
    "s_cselect_b32 s40, 0, s40\n"
    "buffer_store_dwordx4 v[184:187], v2, s[36:39], s53 offen offset:0\n"
    "v_mfma_f32_16x16x32_bf16 v[124:127], a[236:239], a[200:203], v[124:127]\n"
    "v_add_u32 v4, s49, v2\n"
    "v_mfma_f32_16x16x32_bf16 v[156:159], a[236:239], a[204:207], v[156:159]\n"
    "s_waitcnt vmcnt(15) lgkmcnt(6)\n"
    "v_mfma_f32_16x16x32_bf16 v[32:35], a[208:211], a[144:147], v[32:35]\n"
    "ds_read_b64_tr_b16 a[232:233], v3 offset:4288\n"
    "ds_read_b64_tr_b16 a[234:235], v3 offset:4800\n"
    "v_mfma_f32_16x16x32_bf16 v[64:67], a[208:211], a[148:151], v[64:67]\n"
    "ds_read_b64_tr_b16 a[236:237], v3 offset:4320\n"
    "ds_read_b64_tr_b16 a[238:239], v3 offset:4832\n"
    "v_mfma_f32_16x16x32_bf16 v[96:99], a[208:211], a[152:155], v[96:99]\n"
    "buffer_load_dwordx4 a[192:195], v19, s[28:31], 0 offen offset:3072\n"
    "v_accvgpr_read_b32 v188, a40\n"
    "v_mfma_f32_16x16x32_bf16 v[128:131], a[208:211], a[156:159], v[128:131]\n"
    "s_waitcnt lgkmcnt(8)\n"
    "v_accvgpr_read_b32 v189, a41\n"
    "v_mfma_f32_16x16x32_bf16 v[36:39], a[212:215], a[144:147], v[36:39]\n"
    "v_accvgpr_read_b32 v190, a42\n"
    "v_accvgpr_read_b32 v191, a43\n"
    "v_mfma_f32_16x16x32_bf16 v[68:71], a[212:215], a[148:151], v[68:71]\n"
    "s_add_u32 m0, s40, s64\n"
    "ds_read_b64_tr_b16 a[208:209], v3 offset:8448\n"
    "v_mfma_f32_16x16x32_bf16 v[100:103], a[212:215], a[152:155], v[100:103]\n"
    "ds_read_b64_tr_b16 a[210:211], v3 offset:8960\n"
    "v_accvgpr_read_b32 v192, a44\n"
    "v_mfma_f32_16x16x32_bf16 v[132:135], a[212:215], a[156:159], v[132:135]\n"
    "s_waitcnt lgkmcnt(8)\n"
    "v_accvgpr_read_b32 v193, a45\n"
    "v_mfma_f32_16x16x32_bf16 v[40:43], a[216:219], a[144:147], v[40:43]\n"
    "buffer_load_dwordx4 v10, s[20:23], s33 offen lds\n"
    "v_mfma_f32_16x16x32_bf16 v[72:75], a[216:219], a[148:151], v[72:75]\n"
    "ds_read_b64_tr_b16 a[212:213], v3 offset:8480\n"
    "ds_read_b64_tr_b16 a[214:215], v3 offset:8992\n"
    "v_mfma_f32_16x16x32_bf16 v[104:107], a[216:219], a[152:155], v[104:107]\n"
    "buffer_load_dwordx4 a[196:199], v19, s[28:31], s46 offen offset:3072\n"
    "v_accvgpr_read_b32 v194, a46\n"
    "v_mfma_f32_16x16x32_bf16 v[136:139], a[216:219], a[156:159], v[136:139]\n"
    "s_waitcnt lgkmcnt(8)\n"
    "v_accvgpr_read_b32 v195, a47\n"
    "v_mfma_f32_16x16x32_bf16 v[44:47], a[220:223], a[144:147], v[44:47]\n"
    "v_mul_f32 v188, v188, v226\n"
    "v_mul_f32 v189, v189, v226\n"
    "v_mfma_f32_16x16x32_bf16 v[76:79], a[220:223], a[148:151], v[76:79]\n"
    "ds_read_b64_tr_b16 a[216:217], v3 offset:8512\n"
    "ds_read_b64_tr_b16 a[218:219], v3 offset:9024\n"
    "v_mfma_f32_16x16x32_bf16 v[108:111], a[220:223], a[152:155], v[108:111]\n"
    "v_mul_f32 v190, v190, v226\n"
    "v_mul_f32 v191, v191, v226\n"
    "v_mfma_f32_16x16x32_bf16 v[140:143], a[220:223], a[156:159], v[140:143]\n"
    "s_waitcnt lgkmcnt(8)\n"
    "v_mul_f32 v192, v192, v226\n"
    "v_mfma_f32_16x16x32_bf16 v[48:51], a[224:227], a[144:147], v[48:51]\n"
    "v_mul_f32 v193, v193, v226\n"
    "v_mul_f32 v194, v194, v226\n"
    "v_mfma_f32_16x16x32_bf16 v[80:83], a[224:227], a[148:151], v[80:83]\n"
    "ds_read_b64_tr_b16 a[220:221], v3 offset:8544\n"
    "ds_read_b64_tr_b16 a[222:223], v3 offset:9056\n"
    "v_mfma_f32_16x16x32_bf16 v[112:115], a[224:227], a[152:155], v[112:115]\n"
    "s_add_u32 m0, s40, s65\n"
    "buffer_load_dwordx4 a[200:203], v19, s[28:31], s47 offen offset:3072\n"
    "v_mfma_f32_16x16x32_bf16 v[144:147], a[224:227], a[156:159], v[144:147]\n"
    "s_waitcnt lgkmcnt(8)\n"
    "v_mul_f32 v195, v195, v226\n"
    "v_mfma_f32_16x16x32_bf16 v[52:55], a[228:231], a[144:147], v[52:55]\n"
    "buffer_load_dwordx4 v11, s[20:23], s33 offen lds\n"
    "v_mfma_f32_16x16x32_bf16 v[84:87], a[228:231], a[148:151], v[84:87]\n"
    "ds_read_b64_tr_b16 a[224:225], v3 offset:12672\n"
    "ds_read_b64_tr_b16 a[226:227], v3 offset:13184\n"
    "v_mfma_f32_16x16x32_bf16 v[116:119], a[228:231], a[152:155], v[116:119]\n"
    "v_cvt_pk_bf16_f32 v196, v188, v189\n"
    "v_cvt_pk_bf16_f32 v197, v190, v191\n"
    "v_mfma_f32_16x16x32_bf16 v[148:151], a[228:231], a[156:159], v[148:151]\n"
    "s_waitcnt lgkmcnt(8)\n"
    "v_cvt_pk_bf16_f32 v198, v192, v193\n"
    "v_mfma_f32_16x16x32_bf16 v[56:59], a[232:235], a[144:147], v[56:59]\n"
    "v_cvt_pk_bf16_f32 v199, v194, v195\n"
    "buffer_store_dwordx4 v[196:199], v2, s[36:39], s53 offen offset:1024\n"
    "v_mfma_f32_16x16x32_bf16 v[88:91], a[232:235], a[148:151], v[88:91]\n"
    "ds_read_b64_tr_b16 a[228:229], v3 offset:12704\n"
    "ds_read_b64_tr_b16 a[230:231], v3 offset:13216\n"
    "v_mfma_f32_16x16x32_bf16 v[120:123], a[232:235], a[152:155], v[120:123]\n"
    "buffer_load_dwordx4 a[204:207], v19, s[28:31], s48 offen offset:3072\n"
    "v_accvgpr_read_b32 v200, a48\n"
    "v_mfma_f32_16x16x32_bf16 v[152:155], a[232:235], a[156:159], v[152:155]\n"
    "s_waitcnt lgkmcnt(8)\n"
    "v_accvgpr_read_b32 v201, a49\n"
    "v_mfma_f32_16x16x32_bf16 v[60:63], a[236:239], a[144:147], v[60:63]\n"
    "v_accvgpr_read_b32 v202, a50\n"
    "v_accvgpr_read_b32 v203, a51\n"
    "v_mfma_f32_16x16x32_bf16 v[92:95], a[236:239], a[148:151], v[92:95]\n"
    "ds_read_b64_tr_b16 a[232:233], v3 offset:12736\n"
    "ds_read_b64_tr_b16 a[234:235], v3 offset:13248\n"
    "v_mfma_f32_16x16x32_bf16 v[124:127], a[236:239], a[152:155], v[124:127]\n"
    "v_mov_b32 v19, v4\n"
    "v_accvgpr_read_b32 v204, a52\n"
    "v_mfma_f32_16x16x32_bf16 v[156:159], a[236:239], a[156:159], v[156:159]\n"
    "s_waitcnt lgkmcnt(8) vmcnt(14)\n"
    "s_add_u32 m0, s40, s66\n"
    "v_mfma_f32_16x16x32_bf16 v[32:35], a[208:211], a[160:163], v[32:35]\n"
    "buffer_load_dwordx4 v12, s[20:23], s33 offen lds\n"
    "v_mfma_f32_16x16x32_bf16 v[64:67], a[208:211], a[164:167], v[64:67]\n"
    "ds_read_b64_tr_b16 a[236:237], v3 offset:12768\n"
    "ds_read_b64_tr_b16 a[238:239], v3 offset:13280\n"
    "v_mfma_f32_16x16x32_bf16 v[96:99], a[208:211], a[168:171], v[96:99]\n"
    "buffer_load_dwordx4 a[144:147], v4, s[28:31], 0 offen offset:0\n"
    "v_accvgpr_read_b32 v205, a53\n"
    "v_mfma_f32_16x16x32_bf16 v[128:131], a[208:211], a[172:175], v[128:131]\n"
    "s_waitcnt lgkmcnt(8)\n"
    "v_accvgpr_read_b32 v206, a54\n"
    "v_mfma_f32_16x16x32_bf16 v[36:39], a[212:215], a[160:163], v[36:39]\n"
    "v_accvgpr_read_b32 v207, a55\n"
    "v_mul_f32 v200, v200, v226\n"
    "v_mfma_f32_16x16x32_bf16 v[68:71], a[212:215], a[164:167], v[68:71]\n"
    "ds_read_b64_tr_b16 a[208:209], v3 offset:16896\n"
    "ds_read_b64_tr_b16 a[210:211], v3 offset:17408\n"
    "v_mfma_f32_16x16x32_bf16 v[100:103], a[212:215], a[168:171], v[100:103]\n"
    "v_mul_f32 v201, v201, v226\n"
    "v_mul_f32 v202, v202, v226\n"
    "v_mfma_f32_16x16x32_bf16 v[132:135], a[212:215], a[172:175], v[132:135]\n"
    "s_waitcnt lgkmcnt(8)\n"
    "v_mul_f32 v203, v203, v226\n"
    "v_mfma_f32_16x16x32_bf16 v[40:43], a[216:219], a[160:163], v[40:43]\n"
    "v_mul_f32 v204, v204, v226\n"
    "v_mul_f32 v205, v205, v226\n"
    "v_mfma_f32_16x16x32_bf16 v[72:75], a[216:219], a[164:167], v[72:75]\n"
    "ds_read_b64_tr_b16 a[212:213], v3 offset:16928\n"
    "ds_read_b64_tr_b16 a[214:215], v3 offset:17440\n"
    "v_mfma_f32_16x16x32_bf16 v[104:107], a[216:219], a[168:171], v[104:107]\n"
    "buffer_load_dwordx4 a[148:151], v4, s[28:31], s46 offen offset:0\n"
    "v_mul_f32 v206, v206, v226\n"
    "v_mfma_f32_16x16x32_bf16 v[136:139], a[216:219], a[172:175], v[136:139]\n"
    "s_waitcnt lgkmcnt(8)\n"
    "v_mul_f32 v207, v207, v226\n"
    "v_mfma_f32_16x16x32_bf16 v[44:47], a[220:223], a[160:163], v[44:47]\n"
    "s_add_u32 m0, s40, s67\n"
    "v_cvt_pk_bf16_f32 v208, v200, v201\n"
    "v_mfma_f32_16x16x32_bf16 v[76:79], a[220:223], a[164:167], v[76:79]\n"
    "ds_read_b64_tr_b16 a[216:217], v3 offset:16960\n"
    "ds_read_b64_tr_b16 a[218:219], v3 offset:17472\n"
    "v_mfma_f32_16x16x32_bf16 v[108:111], a[220:223], a[168:171], v[108:111]\n"
    "buffer_load_dwordx4 v13, s[20:23], s33 offen lds\n"
    "v_mfma_f32_16x16x32_bf16 v[140:143], a[220:223], a[172:175], v[140:143]\n"
    "s_waitcnt lgkmcnt(8)\n"
    "v_cvt_pk_bf16_f32 v209, v202, v203\n"
    "v_mfma_f32_16x16x32_bf16 v[48:51], a[224:227], a[160:163], v[48:51]\n"
    "v_cvt_pk_bf16_f32 v210, v204, v205\n"
    "v_cvt_pk_bf16_f32 v211, v206, v207\n"
    "v_mfma_f32_16x16x32_bf16 v[80:83], a[224:227], a[164:167], v[80:83]\n"
    "ds_read_b64_tr_b16 a[220:221], v3 offset:16992\n"
    "ds_read_b64_tr_b16 a[222:223], v3 offset:17504\n"
    "v_mfma_f32_16x16x32_bf16 v[112:115], a[224:227], a[168:171], v[112:115]\n"
    "buffer_load_dwordx4 a[152:155], v4, s[28:31], s47 offen offset:0\n"
    "buffer_store_dwordx4 v[208:211], v2, s[36:39], s53 offen offset:2048\n"
    "v_mfma_f32_16x16x32_bf16 v[144:147], a[224:227], a[172:175], v[144:147]\n"
    "s_waitcnt lgkmcnt(8)\n"
    "v_accvgpr_read_b32 v212, a56\n"
    "v_mfma_f32_16x16x32_bf16 v[52:55], a[228:231], a[160:163], v[52:55]\n"
    "v_accvgpr_read_b32 v213, a57\n"
    "v_accvgpr_read_b32 v214, a58\n"
    "v_mfma_f32_16x16x32_bf16 v[84:87], a[228:231], a[164:167], v[84:87]\n"
    "ds_read_b64_tr_b16 a[224:225], v3 offset:21120\n"
    "ds_read_b64_tr_b16 a[226:227], v3 offset:21632\n"
    "v_mfma_f32_16x16x32_bf16 v[116:119], a[228:231], a[168:171], v[116:119]\n"
    "s_sub_u32 s50, s50, 1\n"
    "v_accvgpr_read_b32 v215, a59\n"
    "v_mfma_f32_16x16x32_bf16 v[148:151], a[228:231], a[172:175], v[148:151]\n"
    "s_waitcnt lgkmcnt(8)\n"
    "v_accvgpr_read_b32 v216, a60\n"
    "v_mfma_f32_16x16x32_bf16 v[56:59], a[232:235], a[160:163], v[56:59]\n"
    "v_accvgpr_read_b32 v217, a61\n"
    "v_accvgpr_read_b32 v218, a62\n"
    "v_mfma_f32_16x16x32_bf16 v[88:91], a[232:235], a[164:167], v[88:91]\n"
    "s_add_u32 m0, s40, s68\n"
    "ds_read_b64_tr_b16 a[228:229], v3 offset:21152\n"
    "v_mfma_f32_16x16x32_bf16 v[120:123], a[232:235], a[168:171], v[120:123]\n"
    "ds_read_b64_tr_b16 a[230:231], v3 offset:21664\n"
    "buffer_load_dwordx4 a[156:159], v4, s[28:31], s48 offen offset:0\n"
    "v_mfma_f32_16x16x32_bf16 v[152:155], a[232:235], a[172:175], v[152:155]\n"
    "s_waitcnt lgkmcnt(8)\n"
    "v_accvgpr_read_b32 v219, a63\n"
    "v_mfma_f32_16x16x32_bf16 v[60:63], a[236:239], a[160:163], v[60:63]\n"
    "buffer_load_dwordx4 v14, s[24:27], s33 offen lds\n"
    "v_mfma_f32_16x16x32_bf16 v[92:95], a[236:239], a[164:167], v[92:95]\n"
    "ds_read_b64_tr_b16 a[232:233], v3 offset:21184\n"
    "ds_read_b64_tr_b16 a[234:235], v3 offset:21696\n"
    "v_mfma_f32_16x16x32_bf16 v[124:127], a[236:239], a[168:171], v[124:127]\n"
    "v_mul_f32 v212, v212, v226\n"
    "v_mul_f32 v213, v213, v226\n"
    "v_mfma_f32_16x16x32_bf16 v[156:159], a[236:239], a[172:175], v[156:159]\n"
    "s_waitcnt vmcnt(16) lgkmcnt(8)\n"
    "v_mul_f32 v214, v214, v226\n"
    "v_mfma_f32_16x16x32_bf16 v[32:35], a[208:211], a[176:179], v[32:35]\n"
    "s_add_u32 s49, s49, 4096\n"
    "v_mul_f32 v215, v215, v226\n"
    "v_mfma_f32_16x16x32_bf16 v[64:67], a[208:211], a[180:183], v[64:67]\n"
    "ds_read_b64_tr_b16 a[236:237], v3 offset:21216\n"
    "ds_read_b64_tr_b16 a[238:239], v3 offset:21728\n"
    "v_mfma_f32_16x16x32_bf16 v[96:99], a[208:211], a[184:187], v[96:99]\n"
    "buffer_load_dwordx4 a[160:163], v4, s[28:31], 0 offen offset:1024\n"
    "v_mul_f32 v216, v216, v226\n"
    "v_mfma_f32_16x16x32_bf16 v[128:131], a[208:211], a[188:191], v[128:131]\n"
    "s_waitcnt lgkmcnt(8)\n"
    "v_mul_f32 v217, v217, v226\n"
    "v_mfma_f32_16x16x32_bf16 v[36:39], a[212:215], a[176:179], v[36:39]\n"
    "v_mul_f32 v218, v218, v226\n"
    "v_mul_f32 v219, v219, v226\n"
    "v_mfma_f32_16x16x32_bf16 v[68:71], a[212:215], a[180:183], v[68:71]\n"
    "ds_read_b64_tr_b16 a[208:209], v3 offset:25344\n"
    "ds_read_b64_tr_b16 a[210:211], v3 offset:25856\n"
    "v_mfma_f32_16x16x32_bf16 v[100:103], a[212:215], a[184:187], v[100:103]\n"
    "s_add_u32 m0, s40, s69\n"
    "v_cvt_pk_bf16_f32 v220, v212, v213\n"
    "v_mfma_f32_16x16x32_bf16 v[132:135], a[212:215], a[188:191], v[132:135]\n"
    "s_waitcnt lgkmcnt(8)\n"
    "v_cvt_pk_bf16_f32 v221, v214, v215\n"
    "v_mfma_f32_16x16x32_bf16 v[40:43], a[216:219], a[176:179], v[40:43]\n"
    "buffer_load_dwordx4 v15, s[24:27], s33 offen lds\n"
    "v_mfma_f32_16x16x32_bf16 v[72:75], a[216:219], a[180:183], v[72:75]\n"
    "ds_read_b64_tr_b16 a[212:213], v3 offset:25376\n"
    "ds_read_b64_tr_b16 a[214:215], v3 offset:25888\n"
    "v_mfma_f32_16x16x32_bf16 v[104:107], a[216:219], a[184:187], v[104:107]\n"
    "buffer_load_dwordx4 a[164:167], v4, s[28:31], s46 offen offset:1024\n"
    "v_cvt_pk_bf16_f32 v222, v216, v217\n"
    "v_mfma_f32_16x16x32_bf16 v[136:139], a[216:219], a[188:191], v[136:139]\n"
    "s_waitcnt lgkmcnt(8)\n"
    "v_cvt_pk_bf16_f32 v223, v218, v219\n"
    "v_mfma_f32_16x16x32_bf16 v[44:47], a[220:223], a[176:179], v[44:47]\n"
    "buffer_store_dwordx4 v[220:223], v2, s[36:39], s53 offen offset:3072\n"
    "v_accvgpr_read_b32 v176, a64\n"
    "v_mfma_f32_16x16x32_bf16 v[76:79], a[220:223], a[180:183], v[76:79]\n"
    "ds_read_b64_tr_b16 a[216:217], v3 offset:25408\n"
    "ds_read_b64_tr_b16 a[218:219], v3 offset:25920\n"
    "v_mfma_f32_16x16x32_bf16 v[108:111], a[220:223], a[184:187], v[108:111]\n"
    "s_cmp_eq_u32 s49, 20480\n"
    "s_cselect_b32 s49, 0, s49\n"
    "v_mfma_f32_16x16x32_bf16 v[140:143], a[220:223], a[188:191], v[140:143]\n"
    "s_waitcnt lgkmcnt(8)\n"
    "v_accvgpr_read_b32 v177, a65\n"
    "v_mfma_f32_16x16x32_bf16 v[48:51], a[224:227], a[176:179], v[48:51]\n"
    "v_accvgpr_read_b32 v178, a66\n"
    "v_accvgpr_read_b32 v179, a67\n"
    "v_mfma_f32_16x16x32_bf16 v[80:83], a[224:227], a[180:183], v[80:83]\n"
    "ds_read_b64_tr_b16 a[220:221], v3 offset:25440\n"
    "ds_read_b64_tr_b16 a[222:223], v3 offset:25952\n"
    "v_mfma_f32_16x16x32_bf16 v[112:115], a[224:227], a[184:187], v[112:115]\n"
    "buffer_load_dwordx4 a[168:171], v4, s[28:31], s47 offen offset:1024\n"
    "v_accvgpr_read_b32 v180, a68\n"
    "v_mfma_f32_16x16x32_bf16 v[144:147], a[224:227], a[188:191], v[144:147]\n"
    "s_waitcnt lgkmcnt(8)\n"
    "s_add_u32 m0, s40, s70\n"
    "v_mfma_f32_16x16x32_bf16 v[52:55], a[228:231], a[176:179], v[52:55]\n"
    "v_accvgpr_read_b32 v181, a69\n"
    "v_accvgpr_read_b32 v182, a70\n"
    "v_mfma_f32_16x16x32_bf16 v[84:87], a[228:231], a[180:183], v[84:87]\n"
    "ds_read_b64_tr_b16 a[224:225], v3 offset:29568\n"
    "v_accvgpr_read_b32 v183, a71\n"
    "v_mfma_f32_16x16x32_bf16 v[116:119], a[228:231], a[184:187], v[116:119]\n"
    "buffer_load_dwordx4 v16, s[24:27], s33 offen lds\n"
    "v_mfma_f32_16x16x32_bf16 v[148:151], a[228:231], a[188:191], v[148:151]\n"
    "s_waitcnt lgkmcnt(7)\n"
    "ds_read_b64_tr_b16 a[226:227], v3 offset:30080\n"
    "v_mfma_f32_16x16x32_bf16 v[56:59], a[232:235], a[176:179], v[56:59]\n"
    "v_mul_f32 v176, v176, v228\n"
    "v_mul_f32 v177, v177, v228\n"
    "v_mfma_f32_16x16x32_bf16 v[88:91], a[232:235], a[180:183], v[88:91]\n"
    "ds_read_b64_tr_b16 a[228:229], v3 offset:29600\n"
    "v_mul_f32 v178, v178, v228\n"
    "v_mfma_f32_16x16x32_bf16 v[120:123], a[232:235], a[184:187], v[120:123]\n"
    "ds_read_b64_tr_b16 a[230:231], v3 offset:30112\n"
    "v_mul_f32 v179, v179, v228\n"
    "v_mfma_f32_16x16x32_bf16 v[152:155], a[232:235], a[188:191], v[152:155]\n"
    "buffer_load_dwordx4 a[172:175], v4, s[28:31], s48 offen offset:1024\n"
    "v_mul_f32 v180, v180, v228\n"
    "v_mfma_f32_16x16x32_bf16 v[60:63], a[236:239], a[176:179], v[60:63]\n"
    "v_mul_f32 v181, v181, v228\n"
    "v_mul_f32 v182, v182, v228\n"
    "v_mfma_f32_16x16x32_bf16 v[92:95], a[236:239], a[180:183], v[92:95]\n"
    "ds_read_b64_tr_b16 a[232:233], v3 offset:29632\n"
    "ds_read_b64_tr_b16 a[234:235], v3 offset:30144\n"
    "v_mfma_f32_16x16x32_bf16 v[124:127], a[236:239], a[184:187], v[124:127]\n"
    "v_mul_f32 v183, v183, v228\n"
    "v_cvt_pk_bf16_f32 v184, v176, v177\n"
    "v_mfma_f32_16x16x32_bf16 v[156:159], a[236:239], a[188:191], v[156:159]\n"
    "s_waitcnt vmcnt(15) lgkmcnt(8)\n"
    "v_cvt_pk_bf16_f32 v185, v178, v179\n"
    "v_mfma_f32_16x16x32_bf16 v[32:35], a[208:211], a[192:195], v[32:35]\n"
    "s_add_u32 m0, s40, s71\n"
    "v_cvt_pk_bf16_f32 v186, v180, v181\n"
    "v_mfma_f32_16x16x32_bf16 v[64:67], a[208:211], a[196:199], v[64:67]\n"
    "ds_read_b64_tr_b16 a[236:237], v3 offset:29664\n"
    "ds_read_b64_tr_b16 a[238:239], v3 offset:30176\n"
    "v_mfma_f32_16x16x32_bf16 v[96:99], a[208:211], a[200:203], v[96:99]\n"
    "buffer_load_dwordx4 v17, s[24:27], s33 offen lds\n"
    "v_mfma_f32_16x16x32_bf16 v[128:131], a[208:211], a[204:207], v[128:131]\n"
    "s_barrier\n"
    "buffer_load_dwordx4 a[176:179], v4, s[28:31], 0 offen offset:2048\n"
    "v_mfma_f32_16x16x32_bf16 v[36:39], a[212:215], a[192:195], v[36:39]\n"
    "v_cvt_pk_bf16_f32 v187, v182, v183\n"
    "buffer_store_dwordx4 v[184:187], v2, s[36:39], s54 offen offset:0\n"
    "v_mfma_f32_16x16x32_bf16 v[68:71], a[212:215], a[196:199], v[68:71]\n"
    "v_add_u32 v3, s44, v3\n"
    "v_accvgpr_read_b32 v188, a72\n"
    "v_mfma_f32_16x16x32_bf16 v[100:103], a[212:215], a[200:203], v[100:103]\n"
    "ds_read_b64_tr_b16 a[208:209], v3 offset:0\n"
    "v_accvgpr_read_b32 v189, a73\n"
    "v_mfma_f32_16x16x32_bf16 v[132:135], a[212:215], a[204:207], v[132:135]\n"
    "s_waitcnt lgkmcnt(7)\n"
    "ds_read_b64_tr_b16 a[210:211], v3 offset:512\n"
    "v_mfma_f32_16x16x32_bf16 v[40:43], a[216:219], a[192:195], v[40:43]\n"
    "buffer_load_dwordx4 a[180:183], v4, s[28:31], s46 offen offset:2048\n"
    "v_accvgpr_read_b32 v190, a74\n"
    "v_mfma_f32_16x16x32_bf16 v[72:75], a[216:219], a[196:199], v[72:75]\n"
    "ds_read_b64_tr_b16 a[212:213], v3 offset:32\n"
    "v_accvgpr_read_b32 v191, a75\n"
    "v_mfma_f32_16x16x32_bf16 v[104:107], a[216:219], a[200:203], v[104:107]\n"
    "ds_read_b64_tr_b16 a[214:215], v3 offset:544\n"
    "v_accvgpr_read_b32 v192, a76\n"
    "v_mfma_f32_16x16x32_bf16 v[136:139], a[216:219], a[204:207], v[136:139]\n"
    "s_waitcnt lgkmcnt(8)\n"
    "v_accvgpr_read_b32 v193, a77\n"
    "v_mfma_f32_16x16x32_bf16 v[44:47], a[220:223], a[192:195], v[44:47]\n"
    "v_accvgpr_read_b32 v194, a78\n"
    "v_accvgpr_read_b32 v195, a79\n"
    "v_mfma_f32_16x16x32_bf16 v[76:79], a[220:223], a[196:199], v[76:79]\n"
    "ds_read_b64_tr_b16 a[216:217], v3 offset:64\n"
    "v_mul_f32 v188, v188, v228\n"
    "v_mfma_f32_16x16x32_bf16 v[108:111], a[220:223], a[200:203], v[108:111]\n"
    "ds_read_b64_tr_b16 a[218:219], v3 offset:576\n"
    "buffer_load_dwordx4 a[184:187], v4, s[28:31], s47 offen offset:2048\n"
    "v_mfma_f32_16x16x32_bf16 v[140:143], a[220:223], a[204:207], v[140:143]\n"
    "s_waitcnt lgkmcnt(8)\n"
    "v_mul_f32 v189, v189, v228\n"
    "v_mfma_f32_16x16x32_bf16 v[48:51], a[224:227], a[192:195], v[48:51]\n"
    "v_mul_f32 v190, v190, v228\n"
    "v_mul_f32 v191, v191, v228\n"
    "v_mfma_f32_16x16x32_bf16 v[80:83], a[224:227], a[196:199], v[80:83]\n"
    "ds_read_b64_tr_b16 a[220:221], v3 offset:96\n"
    "ds_read_b64_tr_b16 a[222:223], v3 offset:608\n"
    "v_mfma_f32_16x16x32_bf16 v[112:115], a[224:227], a[200:203], v[112:115]\n"
    "v_mul_f32 v192, v192, v228\n"
    "v_mul_f32 v193, v193, v228\n"
    "v_mfma_f32_16x16x32_bf16 v[144:147], a[224:227], a[204:207], v[144:147]\n"
    "s_waitcnt lgkmcnt(8)\n"
    "v_mul_f32 v194, v194, v228\n"
    "v_mfma_f32_16x16x32_bf16 v[52:55], a[228:231], a[192:195], v[52:55]\n"
    "s_add_u32 s43, s43, s44\n"
    "buffer_load_dwordx4 a[188:191], v4, s[28:31], s48 offen offset:2048\n"
    "v_mfma_f32_16x16x32_bf16 v[84:87], a[228:231], a[196:199], v[84:87]\n"
    "ds_read_b64_tr_b16 a[224:225], v3 offset:4224\n"
    "v_mul_f32 v195, v195, v228\n"
    "v_mfma_f32_16x16x32_bf16 v[116:119], a[228:231], a[200:203], v[116:119]\n"
    "s_add_u32 s33, s33, s41\n"
    "ds_read_b64_tr_b16 a[226:227], v3 offset:4736\n"
    "v_mfma_f32_16x16x32_bf16 v[148:151], a[228:231], a[204:207], v[148:151]\n"
    "s_waitcnt lgkmcnt(8)\n"
    "v_cvt_pk_bf16_f32 v196, v188, v189\n"
    "v_mfma_f32_16x16x32_bf16 v[56:59], a[232:235], a[192:195], v[56:59]\n"
    "s_cmp_eq_u32 s43, 101376\n"
    "s_cselect_b32 s44, -101376, s51\n"
    "v_mfma_f32_16x16x32_bf16 v[88:91], a[232:235], a[196:199], v[88:91]\n"
    "s_add_u32 s40, s40, s51\n"
    "v_cvt_pk_bf16_f32 v197, v190, v191\n"
    "v_mfma_f32_16x16x32_bf16 v[120:123], a[232:235], a[200:203], v[120:123]\n"
    "ds_read_b64_tr_b16 a[228:229], v3 offset:4256\n"
    "v_cvt_pk_bf16_f32 v198, v192, v193\n"
    "v_mfma_f32_16x16x32_bf16 v[152:155], a[232:235], a[204:207], v[152:155]\n"
    "s_waitcnt lgkmcnt(7)\n"
    "v_cvt_pk_bf16_f32 v199, v194, v195\n"
    "v_mfma_f32_16x16x32_bf16 v[60:63], a[236:239], a[192:195], v[60:63]\n"
    "ds_read_b64_tr_b16 a[230:231], v3 offset:4768\n"
    "buffer_store_dwordx4 v[196:199], v2, s[36:39], s54 offen offset:1024\n"
    "v_mfma_f32_16x16x32_bf16 v[92:95], a[236:239], a[196:199], v[92:95]\n"
    "s_cmp_eq_u32 s40, 135168\n"
    "s_cselect_b32 s40, 0, s40\n"
    "v_mfma_f32_16x16x32_bf16 v[124:127], a[236:239], a[200:203], v[124:127]\n"
    "v_add_u32 v4, s49, v2\n"
    "v_mfma_f32_16x16x32_bf16 v[156:159], a[236:239], a[204:207], v[156:159]\n"
    "s_waitcnt vmcnt(15) lgkmcnt(6)\n"
    "v_mfma_f32_16x16x32_bf16 v[32:35], a[208:211], a[144:147], v[32:35]\n"
    "ds_read_b64_tr_b16 a[232:233], v3 offset:4288\n"
    "ds_read_b64_tr_b16 a[234:235], v3 offset:4800\n"
    "v_mfma_f32_16x16x32_bf16 v[64:67], a[208:211], a[148:151], v[64:67]\n"
    "ds_read_b64_tr_b16 a[236:237], v3 offset:4320\n"
    "ds_read_b64_tr_b16 a[238:239], v3 offset:4832\n"
    "v_mfma_f32_16x16x32_bf16 v[96:99], a[208:211], a[152:155], v[96:99]\n"
    "buffer_load_dwordx4 a[192:195], v19, s[28:31], 0 offen offset:3072\n"
    "v_accvgpr_read_b32 v200, a80\n"
    "v_mfma_f32_16x16x32_bf16 v[128:131], a[208:211], a[156:159], v[128:131]\n"
    "s_waitcnt lgkmcnt(8)\n"
    "v_accvgpr_read_b32 v201, a81\n"
    "v_mfma_f32_16x16x32_bf16 v[36:39], a[212:215], a[144:147], v[36:39]\n"
    "v_accvgpr_read_b32 v202, a82\n"
    "v_accvgpr_read_b32 v203, a83\n"
    "v_mfma_f32_16x16x32_bf16 v[68:71], a[212:215], a[148:151], v[68:71]\n"
    "s_add_u32 m0, s40, s64\n"
    "ds_read_b64_tr_b16 a[208:209], v3 offset:8448\n"
    "v_mfma_f32_16x16x32_bf16 v[100:103], a[212:215], a[152:155], v[100:103]\n"
    "ds_read_b64_tr_b16 a[210:211], v3 offset:8960\n"
    "v_accvgpr_read_b32 v204, a84\n"
    "v_mfma_f32_16x16x32_bf16 v[132:135], a[212:215], a[156:159], v[132:135]\n"
    "s_waitcnt lgkmcnt(8)\n"
    "v_accvgpr_read_b32 v205, a85\n"
    "v_mfma_f32_16x16x32_bf16 v[40:43], a[216:219], a[144:147], v[40:43]\n"
    "buffer_load_dwordx4 v10, s[20:23], s33 offen lds\n"
    "v_mfma_f32_16x16x32_bf16 v[72:75], a[216:219], a[148:151], v[72:75]\n"
    "ds_read_b64_tr_b16 a[212:213], v3 offset:8480\n"
    "ds_read_b64_tr_b16 a[214:215], v3 offset:8992\n"
    "v_mfma_f32_16x16x32_bf16 v[104:107], a[216:219], a[152:155], v[104:107]\n"
    "buffer_load_dwordx4 a[196:199], v19, s[28:31], s46 offen offset:3072\n"
    "v_accvgpr_read_b32 v206, a86\n"
    "v_mfma_f32_16x16x32_bf16 v[136:139], a[216:219], a[156:159], v[136:139]\n"
    "s_waitcnt lgkmcnt(8)\n"
    "v_accvgpr_read_b32 v207, a87\n"
    "v_mfma_f32_16x16x32_bf16 v[44:47], a[220:223], a[144:147], v[44:47]\n"
    "v_mul_f32 v200, v200, v228\n"
    "v_mul_f32 v201, v201, v228\n"
    "v_mfma_f32_16x16x32_bf16 v[76:79], a[220:223], a[148:151], v[76:79]\n"
    "ds_read_b64_tr_b16 a[216:217], v3 offset:8512\n"
    "ds_read_b64_tr_b16 a[218:219], v3 offset:9024\n"
    "v_mfma_f32_16x16x32_bf16 v[108:111], a[220:223], a[152:155], v[108:111]\n"
    "v_mul_f32 v202, v202, v228\n"
    "v_mul_f32 v203, v203, v228\n"
    "v_mfma_f32_16x16x32_bf16 v[140:143], a[220:223], a[156:159], v[140:143]\n"
    "s_waitcnt lgkmcnt(8)\n"
    "v_mul_f32 v204, v204, v228\n"
    "v_mfma_f32_16x16x32_bf16 v[48:51], a[224:227], a[144:147], v[48:51]\n"
    "v_mul_f32 v205, v205, v228\n"
    "v_mul_f32 v206, v206, v228\n"
    "v_mfma_f32_16x16x32_bf16 v[80:83], a[224:227], a[148:151], v[80:83]\n"
    "ds_read_b64_tr_b16 a[220:221], v3 offset:8544\n"
    "ds_read_b64_tr_b16 a[222:223], v3 offset:9056\n"
    "v_mfma_f32_16x16x32_bf16 v[112:115], a[224:227], a[152:155], v[112:115]\n"
    "s_add_u32 m0, s40, s65\n"
    "buffer_load_dwordx4 a[200:203], v19, s[28:31], s47 offen offset:3072\n"
    "v_mfma_f32_16x16x32_bf16 v[144:147], a[224:227], a[156:159], v[144:147]\n"
    "s_waitcnt lgkmcnt(8)\n"
    "v_mul_f32 v207, v207, v228\n"
    "v_mfma_f32_16x16x32_bf16 v[52:55], a[228:231], a[144:147], v[52:55]\n"
    "buffer_load_dwordx4 v11, s[20:23], s33 offen lds\n"
    "v_mfma_f32_16x16x32_bf16 v[84:87], a[228:231], a[148:151], v[84:87]\n"
    "ds_read_b64_tr_b16 a[224:225], v3 offset:12672\n"
    "ds_read_b64_tr_b16 a[226:227], v3 offset:13184\n"
    "v_mfma_f32_16x16x32_bf16 v[116:119], a[228:231], a[152:155], v[116:119]\n"
    "v_cvt_pk_bf16_f32 v208, v200, v201\n"
    "v_cvt_pk_bf16_f32 v209, v202, v203\n"
    "v_mfma_f32_16x16x32_bf16 v[148:151], a[228:231], a[156:159], v[148:151]\n"
    "s_waitcnt lgkmcnt(8)\n"
    "v_cvt_pk_bf16_f32 v210, v204, v205\n"
    "v_mfma_f32_16x16x32_bf16 v[56:59], a[232:235], a[144:147], v[56:59]\n"
    "v_cvt_pk_bf16_f32 v211, v206, v207\n"
    "buffer_store_dwordx4 v[208:211], v2, s[36:39], s54 offen offset:2048\n"
    "v_mfma_f32_16x16x32_bf16 v[88:91], a[232:235], a[148:151], v[88:91]\n"
    "ds_read_b64_tr_b16 a[228:229], v3 offset:12704\n"
    "ds_read_b64_tr_b16 a[230:231], v3 offset:13216\n"
    "v_mfma_f32_16x16x32_bf16 v[120:123], a[232:235], a[152:155], v[120:123]\n"
    "buffer_load_dwordx4 a[204:207], v19, s[28:31], s48 offen offset:3072\n"
    "v_accvgpr_read_b32 v212, a88\n"
    "v_mfma_f32_16x16x32_bf16 v[152:155], a[232:235], a[156:159], v[152:155]\n"
    "s_waitcnt lgkmcnt(8)\n"
    "v_accvgpr_read_b32 v213, a89\n"
    "v_mfma_f32_16x16x32_bf16 v[60:63], a[236:239], a[144:147], v[60:63]\n"
    "v_accvgpr_read_b32 v214, a90\n"
    "v_accvgpr_read_b32 v215, a91\n"
    "v_mfma_f32_16x16x32_bf16 v[92:95], a[236:239], a[148:151], v[92:95]\n"
    "ds_read_b64_tr_b16 a[232:233], v3 offset:12736\n"
    "ds_read_b64_tr_b16 a[234:235], v3 offset:13248\n"
    "v_mfma_f32_16x16x32_bf16 v[124:127], a[236:239], a[152:155], v[124:127]\n"
    "v_mov_b32 v19, v4\n"
    "v_accvgpr_read_b32 v216, a92\n"
    "v_mfma_f32_16x16x32_bf16 v[156:159], a[236:239], a[156:159], v[156:159]\n"
    "s_waitcnt lgkmcnt(8) vmcnt(14)\n"
    "s_add_u32 m0, s40, s66\n"
    "v_mfma_f32_16x16x32_bf16 v[32:35], a[208:211], a[160:163], v[32:35]\n"
    "v_accvgpr_read_b32 v217, a93\n"
    "v_accvgpr_read_b32 v218, a94\n"
    "v_mfma_f32_16x16x32_bf16 v[64:67], a[208:211], a[164:167], v[64:67]\n"
    "ds_read_b64_tr_b16 a[236:237], v3 offset:12768\n"
    "v_accvgpr_read_b32 v219, a95\n"
    "v_mfma_f32_16x16x32_bf16 v[96:99], a[208:211], a[168:171], v[96:99]\n"
    "buffer_load_dwordx4 v12, s[20:23], s33 offen lds\n"
    "v_mfma_f32_16x16x32_bf16 v[128:131], a[208:211], a[172:175], v[128:131]\n"
    "s_waitcnt lgkmcnt(7)\n"
    "ds_read_b64_tr_b16 a[238:239], v3 offset:13280\n"
    "v_mfma_f32_16x16x32_bf16 v[36:39], a[212:215], a[160:163], v[36:39]\n"
    "buffer_load_dwordx4 a[144:147], v4, s[28:31], 0 offen offset:0\n"
    "v_mul_f32 v212, v212, v228\n"
    "v_mfma_f32_16x16x32_bf16 v[68:71], a[212:215], a[164:167], v[68:71]\n"
    "v_mul_f32 v213, v213, v228\n"
    "v_mul_f32 v214, v214, v228\n"
    "v_mfma_f32_16x16x32_bf16 v[100:103], a[212:215], a[168:171], v[100:103]\n"
    "ds_read_b64_tr_b16 a[208:209], v3 offset:16896\n"
    "v_mul_f32 v215, v215, v228\n"
    "v_mfma_f32_16x16x32_bf16 v[132:135], a[212:215], a[172:175], v[132:135]\n"
    "s_waitcnt lgkmcnt(7)\n"
    "ds_read_b64_tr_b16 a[210:211], v3 offset:17408\n"
    "v_mfma_f32_16x16x32_bf16 v[40:43], a[216:219], a[160:163], v[40:43]\n"
    "v_mul_f32 v216, v216, v228\n"
    "v_mul_f32 v217, v217, v228\n"
    "v_mfma_f32_16x16x32_bf16 v[72:75], a[216:219], a[164:167], v[72:75]\n"
    "ds_read_b64_tr_b16 a[212:213], v3 offset:16928\n"
    "v_mul_f32 v218, v218, v228\n"
    "v_mfma_f32_16x16x32_bf16 v[104:107], a[216:219], a[168:171], v[104:107]\n"
    "ds_read_b64_tr_b16 a[214:215], v3 offset:17440\n"
    "v_mul_f32 v219, v219, v228\n"
    "v_mfma_f32_16x16x32_bf16 v[136:139], a[216:219], a[172:175], v[136:139]\n"
    "s_waitcnt lgkmcnt(8)\n"
    "buffer_load_dwordx4 a[148:151], v4, s[28:31], s46 offen offset:0\n"
    "v_mfma_f32_16x16x32_bf16 v[44:47], a[220:223], a[160:163], v[44:47]\n"
    "v_cvt_pk_bf16_f32 v220, v212, v213\n"
    "v_cvt_pk_bf16_f32 v221, v214, v215\n"
    "v_mfma_f32_16x16x32_bf16 v[76:79], a[220:223], a[164:167], v[76:79]\n"
    "s_add_u32 m0, s40, s67\n"
    "v_cvt_pk_bf16_f32 v222, v216, v217\n"
    "v_mfma_f32_16x16x32_bf16 v[108:111], a[220:223], a[168:171], v[108:111]\n"
    "ds_read_b64_tr_b16 a[216:217], v3 offset:16960\n"
    "v_cvt_pk_bf16_f32 v223, v218, v219\n"
    "v_mfma_f32_16x16x32_bf16 v[140:143], a[220:223], a[172:175], v[140:143]\n"
    "s_waitcnt lgkmcnt(7)\n"
    "buffer_store_dwordx4 v[220:223], v2, s[36:39], s54 offen offset:3072\n"
    "v_mfma_f32_16x16x32_bf16 v[48:51], a[224:227], a[160:163], v[48:51]\n"
    "ds_read_b64_tr_b16 a[218:219], v3 offset:17472\n"
    "v_accvgpr_read_b32 v176, a96\n"
    "v_mfma_f32_16x16x32_bf16 v[80:83], a[224:227], a[164:167], v[80:83]\n"
    "buffer_load_dwordx4 v13, s[20:23], s33 offen lds\n"
    "v_mfma_f32_16x16x32_bf16 v[112:115], a[224:227], a[168:171], v[112:115]\n"
    "v_accvgpr_read_b32 v177, a97\n"
    "v_accvgpr_read_b32 v178, a98\n"
    "v_mfma_f32_16x16x32_bf16 v[144:147], a[224:227], a[172:175], v[144:147]\n"
    "s_waitcnt lgkmcnt(6)\n"
    "ds_read_b64_tr_b16 a[220:221], v3 offset:16992\n"
    "v_mfma_f32_16x16x32_bf16 v[52:55], a[228:231], a[160:163], v[52:55]\n"
    "ds_read_b64_tr_b16 a[222:223], v3 offset:17504\n"
    "v_accvgpr_read_b32 v179, a99\n"
    "v_mfma_f32_16x16x32_bf16 v[84:87], a[228:231], a[164:167], v[84:87]\n"
    "buffer_load_dwordx4 a[152:155], v4, s[28:31], s47 offen offset:0\n"
    "v_accvgpr_read_b32 v180, a100\n"
    "v_mfma_f32_16x16x32_bf16 v[116:119], a[228:231], a[168:171], v[116:119]\n"
    "v_accvgpr_read_b32 v181, a101\n"
    "v_accvgpr_read_b32 v182, a102\n"
    "v_mfma_f32_16x16x32_bf16 v[148:151], a[228:231], a[172:175], v[148:151]\n"
    "s_waitcnt lgkmcnt(6)\n"
    "ds_read_b64_tr_b16 a[224:225], v3 offset:21120\n"
    "v_mfma_f32_16x16x32_bf16 v[56:59], a[232:235], a[160:163], v[56:59]\n"
    "ds_read_b64_tr_b16 a[226:227], v3 offset:21632\n"
    "v_accvgpr_read_b32 v183, a103\n"
    "v_mfma_f32_16x16x32_bf16 v[88:91], a[232:235], a[164:167], v[88:91]\n"
    "v_mul_f32 v176, v176, v230\n"
    "v_mul_f32 v177, v177, v230\n"
    "v_mfma_f32_16x16x32_bf16 v[120:123], a[232:235], a[168:171], v[120:123]\n"
    "v_mul_f32 v178, v178, v230\n"
    "v_mul_f32 v179, v179, v230\n"
    "v_mfma_f32_16x16x32_bf16 v[152:155], a[232:235], a[172:175], v[152:155]\n"
    "s_waitcnt lgkmcnt(6)\n"
    "ds_read_b64_tr_b16 a[228:229], v3 offset:21152\n"
    "v_mfma_f32_16x16x32_bf16 v[60:63], a[236:239], a[160:163], v[60:63]\n"
    "s_add_u32 m0, s40, s68\n"
    "s_sub_u32 s50, s50, 1\n"
    "v_mul_f32 v180, v180, v230\n"
    "v_mfma_f32_16x16x32_bf16 v[92:95], a[236:239], a[164:167], v[92:95]\n"
    "ds_read_b64_tr_b16 a[230:231], v3 offset:21664\n"
    "buffer_load_dwordx4 a[156:159], v4, s[28:31], s48 offen offset:0\n"
    "v_mul_f32 v181, v181, v230\n"
    "v_mfma_f32_16x16x32_bf16 v[124:127], a[236:239], a[168:171], v[124:127]\n"
    "buffer_load_dwordx4 v14, s[24:27], s33 offen lds\n"
    "v_mul_f32 v182, v182, v230\n"
    "v_mfma_f32_16x16x32_bf16 v[156:159], a[236:239], a[172:175], v[156:159]\n"
    "s_waitcnt vmcnt(16) lgkmcnt(6)\n"
    "v_mul_f32 v183, v183, v230\n"
    "v_mfma_f32_16x16x32_bf16 v[32:35], a[208:211], a[176:179], v[32:35]\n"
    "ds_read_b64_tr_b16 a[232:233], v3 offset:21184\n"
    "v_cvt_pk_bf16_f32 v184, v176, v177\n"
    "v_mfma_f32_16x16x32_bf16 v[64:67], a[208:211], a[180:183], v[64:67]\n"
    "ds_read_b64_tr_b16 a[234:235], v3 offset:21696\n"
    "v_cvt_pk_bf16_f32 v185, v178, v179\n"
    "v_mfma_f32_16x16x32_bf16 v[96:99], a[208:211], a[184:187], v[96:99]\n"
    "v_cvt_pk_bf16_f32 v186, v180, v181\n"
    "v_cvt_pk_bf16_f32 v187, v182, v183\n"
    "v_mfma_f32_16x16x32_bf16 v[128:131], a[208:211], a[188:191], v[128:131]\n"
    "s_waitcnt lgkmcnt(6)\n"
    "ds_read_b64_tr_b16 a[236:237], v3 offset:21216\n"
    "v_mfma_f32_16x16x32_bf16 v[36:39], a[212:215], a[176:179], v[36:39]\n"
    "buffer_store_dwordx4 v[184:187], v2, s[36:39], s55 offen offset:0\n"
    "ds_read_b64_tr_b16 a[238:239], v3 offset:21728\n"
    "v_mfma_f32_16x16x32_bf16 v[68:71], a[212:215], a[180:183], v[68:71]\n"
    "buffer_load_dwordx4 a[160:163], v4, s[28:31], 0 offen offset:1024\n"
    "v_accvgpr_read_b32 v188, a104\n"
    "v_accvgpr_read_b32 v189, a105\n"
    "v_mfma_f32_16x16x32_bf16 v[100:103], a[212:215], a[184:187], v[100:103]\n"
    "s_add_u32 s49, s49, 4096\n"
    "v_accvgpr_read_b32 v190, a106\n"
    "v_mfma_f32_16x16x32_bf16 v[132:135], a[212:215], a[188:191], v[132:135]\n"
    "s_waitcnt lgkmcnt(6)\n"
    "v_accvgpr_read_b32 v191, a107\n"
    "v_mfma_f32_16x16x32_bf16 v[40:43], a[216:219], a[176:179], v[40:43]\n"
    "ds_read_b64_tr_b16 a[208:209], v3 offset:25344\n"
    "v_accvgpr_read_b32 v192, a108\n"
    "v_mfma_f32_16x16x32_bf16 v[72:75], a[216:219], a[180:183], v[72:75]\n"
    "s_add_u32 m0, s40, s69\n"
    "ds_read_b64_tr_b16 a[210:211], v3 offset:25856\n"
    "v_accvgpr_read_b32 v193, a109\n"
    "v_mfma_f32_16x16x32_bf16 v[104:107], a[216:219], a[184:187], v[104:107]\n"
    "v_accvgpr_read_b32 v194, a110\n"
    "v_accvgpr_read_b32 v195, a111\n"
    "v_mfma_f32_16x16x32_bf16 v[136:139], a[216:219], a[188:191], v[136:139]\n"
    "s_waitcnt lgkmcnt(6)\n"
    "buffer_load_dwordx4 v15, s[24:27], s33 offen lds\n"
    "v_mfma_f32_16x16x32_bf16 v[44:47], a[220:223], a[176:179], v[44:47]\n"
    "ds_read_b64_tr_b16 a[212:213], v3 offset:25376\n"
    "v_mul_f32 v188, v188, v230\n"
    "v_mfma_f32_16x16x32_bf16 v[76:79], a[220:223], a[180:183], v[76:79]\n"
    "ds_read_b64_tr_b16 a[214:215], v3 offset:25888\n"
    "buffer_load_dwordx4 a[164:167], v4, s[28:31], s46 offen offset:1024\n"
    "v_mul_f32 v189, v189, v230\n"
    "v_mfma_f32_16x16x32_bf16 v[108:111], a[220:223], a[184:187], v[108:111]\n"
    "v_mul_f32 v190, v190, v230\n"
    "v_mul_f32 v191, v191, v230\n"
    "v_mfma_f32_16x16x32_bf16 v[140:143], a[220:223], a[188:191], v[140:143]\n"
    "s_waitcnt lgkmcnt(6)\n"
    "ds_read_b64_tr_b16 a[216:217], v3 offset:25408\n"
    "v_mfma_f32_16x16x32_bf16 v[48:51], a[224:227], a[176:179], v[48:51]\n"
    "s_cmp_eq_u32 s49, 20480\n"
    "s_cselect_b32 s49, 0, s49\n"
    "v_mul_f32 v192, v192, v230\n"
    "v_mul_f32 v193, v193, v230\n"
    "v_mfma_f32_16x16x32_bf16 v[80:83], a[224:227], a[180:183], v[80:83]\n"
    "ds_read_b64_tr_b16 a[218:219], v3 offset:25920\n"
    "v_mul_f32 v194, v194, v230\n"
    "v_mfma_f32_16x16x32_bf16 v[112:115], a[224:227], a[184:187], v[112:115]\n"
    "v_mul_f32 v195, v195, v230\n"
    "v_cvt_pk_bf16_f32 v196, v188, v189\n"
    "v_mfma_f32_16x16x32_bf16 v[144:147], a[224:227], a[188:191], v[144:147]\n"
    "s_waitcnt lgkmcnt(6)\n"
    "ds_read_b64_tr_b16 a[220:221], v3 offset:25440\n"
    "v_mfma_f32_16x16x32_bf16 v[52:55], a[228:231], a[176:179], v[52:55]\n"
    "ds_read_b64_tr_b16 a[222:223], v3 offset:25952\n"
    "v_cvt_pk_bf16_f32 v197, v190, v191\n"
    "v_mfma_f32_16x16x32_bf16 v[84:87], a[228:231], a[180:183], v[84:87]\n"
    "buffer_load_dwordx4 a[168:171], v4, s[28:31], s47 offen offset:1024\n"
    "v_cvt_pk_bf16_f32 v198, v192, v193\n"
    "v_mfma_f32_16x16x32_bf16 v[116:119], a[228:231], a[184:187], v[116:119]\n"
    "s_add_u32 m0, s40, s70\n"
    "v_cvt_pk_bf16_f32 v199, v194, v195\n"
    "v_mfma_f32_16x16x32_bf16 v[148:151], a[228:231], a[188:191], v[148:151]\n"
    "s_waitcnt lgkmcnt(6)\n"
    "buffer_store_dwordx4 v[196:199], v2, s[36:39], s55 offen offset:1024\n"
    "v_mfma_f32_16x16x32_bf16 v[56:59], a[232:235], a[176:179], v[56:59]\n"
    "ds_read_b64_tr_b16 a[224:225], v3 offset:29568\n"
    "buffer_load_dwordx4 v16, s[24:27], s33 offen lds\n"
    "v_accvgpr_read_b32 v200, a112\n"
    "v_mfma_f32_16x16x32_bf16 v[88:91], a[232:235], a[180:183], v[88:91]\n"
    "ds_read_b64_tr_b16 a[226:227], v3 offset:30080\n"
    "v_accvgpr_read_b32 v201, a113\n"
    "v_mfma_f32_16x16x32_bf16 v[120:123], a[232:235], a[184:187], v[120:123]\n"
    "v_accvgpr_read_b32 v202, a114\n"
    "v_accvgpr_read_b32 v203, a115\n"
    "v_mfma_f32_16x16x32_bf16 v[152:155], a[232:235], a[188:191], v[152:155]\n"
    "ds_read_b64_tr_b16 a[228:229], v3 offset:29600\n"
    "v_accvgpr_read_b32 v204, a116\n"
    "v_mfma_f32_16x16x32_bf16 v[60:63], a[236:239], a[176:179], v[60:63]\n"
    "ds_read_b64_tr_b16 a[230:231], v3 offset:30112\n"
    "v_accvgpr_read_b32 v205, a117\n"
    "v_mfma_f32_16x16x32_bf16 v[92:95], a[236:239], a[180:183], v[92:95]\n"
    "buffer_load_dwordx4 a[172:175], v4, s[28:31], s48 offen offset:1024\n"
    "v_accvgpr_read_b32 v206, a118\n"
    "v_mfma_f32_16x16x32_bf16 v[124:127], a[236:239], a[184:187], v[124:127]\n"
    "v_accvgpr_read_b32 v207, a119\n"
    "v_mul_f32 v200, v200, v230\n"
    "v_mfma_f32_16x16x32_bf16 v[156:159], a[236:239], a[188:191], v[156:159]\n"
    "s_waitcnt vmcnt(16) lgkmcnt(6)\n"
    "ds_read_b64_tr_b16 a[232:233], v3 offset:29632\n"
    "v_mfma_f32_16x16x32_bf16 v[32:35], a[208:211], a[192:195], v[32:35]\n"
    "ds_read_b64_tr_b16 a[234:235], v3 offset:30144\n"
    "v_mul_f32 v201, v201, v230\n"
    "v_mfma_f32_16x16x32_bf16 v[64:67], a[208:211], a[196:199], v[64:67]\n"
    "v_mul_f32 v202, v202, v230\n"
    "v_mul_f32 v203, v203, v230\n"
    "v_mfma_f32_16x16x32_bf16 v[96:99], a[208:211], a[200:203], v[96:99]\n"
    "s_add_u32 m0, s40, s71\n"
    "v_mul_f32 v204, v204, v230\n"
    "v_mfma_f32_16x16x32_bf16 v[128:131], a[208:211], a[204:207], v[128:131]\n"
    "s_barrier\n"
    "ds_read_b64_tr_b16 a[236:237], v3 offset:29664\n"
    "v_mfma_f32_16x16x32_bf16 v[36:39], a[212:215], a[192:195], v[36:39]\n"
    "ds_read_b64_tr_b16 a[238:239], v3 offset:30176\n"
    "v_mul_f32 v205, v205, v230\n"
    "v_mfma_f32_16x16x32_bf16 v[68:71], a[212:215], a[196:199], v[68:71]\n"
    "buffer_load_dwordx4 v17, s[24:27], s33 offen lds\n"
    "buffer_load_dwordx4 a[176:179], v4, s[28:31], 0 offen offset:2048\n"
    "v_mul_f32 v206, v206, v230\n"
    "v_mfma_f32_16x16x32_bf16 v[100:103], a[212:215], a[200:203], v[100:103]\n"
    "v_mul_f32 v207, v207, v230\n"
    "v_add_u32 v3, s44, v3\n"
    "v_mfma_f32_16x16x32_bf16 v[132:135], a[212:215], a[204:207], v[132:135]\n"
    "s_waitcnt lgkmcnt(6)\n"
    "v_cvt_pk_bf16_f32 v208, v200, v201\n"
    "v_cvt_pk_bf16_f32 v209, v202, v203\n"
    "v_mfma_f32_16x16x32_bf16 v[40:43], a[216:219], a[192:195], v[40:43]\n"
    "ds_read_b64_tr_b16 a[208:209], v3 offset:0\n"
    "v_cvt_pk_bf16_f32 v210, v204, v205\n"
    "v_mfma_f32_16x16x32_bf16 v[72:75], a[216:219], a[196:199], v[72:75]\n"
    "ds_read_b64_tr_b16 a[210:211], v3 offset:512\n"
    "v_cvt_pk_bf16_f32 v211, v206, v207\n"
    "v_mfma_f32_16x16x32_bf16 v[104:107], a[216:219], a[200:203], v[104:107]\n"
    "buffer_store_dwordx4 v[208:211], v2, s[36:39], s55 offen offset:2048\n"
    "buffer_load_dwordx4 a[180:183], v4, s[28:31], s46 offen offset:2048\n"
    "v_mfma_f32_16x16x32_bf16 v[136:139], a[216:219], a[204:207], v[136:139]\n"
    "s_waitcnt lgkmcnt(6)\n"
    "v_accvgpr_read_b32 v212, a120\n"
    "v_mfma_f32_16x16x32_bf16 v[44:47], a[220:223], a[192:195], v[44:47]\n"
    "ds_read_b64_tr_b16 a[212:213], v3 offset:32\n"
    "v_accvgpr_read_b32 v213, a121\n"
    "v_mfma_f32_16x16x32_bf16 v[76:79], a[220:223], a[196:199], v[76:79]\n"
    "ds_read_b64_tr_b16 a[214:215], v3 offset:544\n"
    "v_accvgpr_read_b32 v214, a122\n"
    "v_mfma_f32_16x16x32_bf16 v[108:111], a[220:223], a[200:203], v[108:111]\n"
    "v_accvgpr_read_b32 v215, a123\n"
    "v_accvgpr_read_b32 v216, a124\n"
    "v_mfma_f32_16x16x32_bf16 v[140:143], a[220:223], a[204:207], v[140:143]\n"
    "s_waitcnt lgkmcnt(6)\n"
    "ds_read_b64_tr_b16 a[216:217], v3 offset:64\n"
    "v_mfma_f32_16x16x32_bf16 v[48:51], a[224:227], a[192:195], v[48:51]\n"
    "ds_read_b64_tr_b16 a[218:219], v3 offset:576\n"
    "v_accvgpr_read_b32 v217, a125\n"
    "v_mfma_f32_16x16x32_bf16 v[80:83], a[224:227], a[196:199], v[80:83]\n"
    "buffer_load_dwordx4 a[184:187], v4, s[28:31], s47 offen offset:2048\n"
    "v_accvgpr_read_b32 v218, a126\n"
    "v_mfma_f32_16x16x32_bf16 v[112:115], a[224:227], a[200:203], v[112:115]\n"
    "v_accvgpr_read_b32 v219, a127\n"
    "v_mul_f32 v212, v212, v230\n"
    "v_mfma_f32_16x16x32_bf16 v[144:147], a[224:227], a[204:207], v[144:147]\n"
    "s_waitcnt lgkmcnt(6)\n"
    "v_mul_f32 v213, v213, v230\n"
    "v_mfma_f32_16x16x32_bf16 v[52:55], a[228:231], a[192:195], v[52:55]\n"
    "ds_read_b64_tr_b16 a[220:221], v3 offset:96\n"
    "v_mul_f32 v214, v214, v230\n"
    "v_mfma_f32_16x16x32_bf16 v[84:87], a[228:231], a[196:199], v[84:87]\n"
    "ds_read_b64_tr_b16 a[222:223], v3 offset:608\n"
    "v_mul_f32 v215, v215, v230\n"
    "v_mfma_f32_16x16x32_bf16 v[116:119], a[228:231], a[200:203], v[116:119]\n"
    "buffer_load_dwordx4 a[188:191], v4, s[28:31], s48 offen offset:2048\n"
    "v_mul_f32 v216, v216, v230\n"
    "v_mfma_f32_16x16x32_bf16 v[148:151], a[228:231], a[204:207], v[148:151]\n"
    "ds_read_b64_tr_b16 a[224:225], v3 offset:4224\n"
    "v_mul_f32 v217, v217, v230\n"
    "v_mfma_f32_16x16x32_bf16 v[56:59], a[232:235], a[192:195], v[56:59]\n"
    "s_add_u32 s43, s43, s44\n"
    "v_mul_f32 v218, v218, v230\n"
    "v_mfma_f32_16x16x32_bf16 v[88:91], a[232:235], a[196:199], v[88:91]\n"
    "s_add_u32 s33, s33, s41\n"
    "ds_read_b64_tr_b16 a[226:227], v3 offset:4736\n"
    "v_mul_f32 v219, v219, v230\n"
    "v_mfma_f32_16x16x32_bf16 v[120:123], a[232:235], a[200:203], v[120:123]\n"
    "s_cmp_eq_u32 s43, 101376\n"
    "s_cselect_b32 s44, -101376, s51\n"
    "v_cvt_pk_bf16_f32 v220, v212, v213\n"
    "v_mfma_f32_16x16x32_bf16 v[152:155], a[232:235], a[204:207], v[152:155]\n"
    "s_waitcnt lgkmcnt(6)\n"
    "s_add_u32 s40, s40, s51\n"
    "v_cvt_pk_bf16_f32 v221, v214, v215\n"
    "v_mfma_f32_16x16x32_bf16 v[60:63], a[236:239], a[192:195], v[60:63]\n"
    "ds_read_b64_tr_b16 a[228:229], v3 offset:4256\n"
    "v_cvt_pk_bf16_f32 v222, v216, v217\n"
    "v_mfma_f32_16x16x32_bf16 v[92:95], a[236:239], a[196:199], v[92:95]\n"
    "s_cmp_eq_u32 s40, 135168\n"
    "s_cselect_b32 s40, 0, s40\n"
    "ds_read_b64_tr_b16 a[230:231], v3 offset:4768\n"
    "v_cvt_pk_bf16_f32 v223, v218, v219\n"
    "v_mfma_f32_16x16x32_bf16 v[124:127], a[236:239], a[200:203], v[124:127]\n"
    "buffer_store_dwordx4 v[220:223], v2, s[36:39], s55 offen offset:3072\n"
    "v_add_u32 v4, s49, v2\n"
    "v_mfma_f32_16x16x32_bf16 v[156:159], a[236:239], a[204:207], v[156:159]\n"
    "s_mov_b32 s22, 0\n"
    "s_mov_b32 s26, 0\n"
    "s_mov_b32 s50, 2\n"
    "K1_F:\n"
    "s_waitcnt vmcnt(16)\n"
    "v_mfma_f32_16x16x32_bf16 v[32:35], a[208:211], a[144:147], v[32:35]\n"
    "s_waitcnt vmcnt(12)\n"
    "ds_read_b64_tr_b16 a[232:233], v3 offset:4288\n"
    "v_mfma_f32_16x16x32_bf16 v[64:67], a[208:211], a[148:151], v[64:67]\n"
    "ds_read_b64_tr_b16 a[234:235], v3 offset:4800\n"
    "ds_read_b64_tr_b16 a[236:237], v3 offset:4320\n"
    "v_mfma_f32_16x16x32_bf16 v[96:99], a[208:211], a[152:155], v[96:99]\n"
    "ds_read_b64_tr_b16 a[238:239], v3 offset:4832\n"
    "buffer_load_dwordx4 a[192:195], v19, s[28:31], 0 offen offset:3072\n"
    "v_mfma_f32_16x16x32_bf16 v[128:131], a[208:211], a[156:159], v[128:131]\n"
    "s_waitcnt lgkmcnt(8)\n"
    "s_sub_u32 s50, s50, 1\n"
    "v_mfma_f32_16x16x32_bf16 v[36:39], a[212:215], a[144:147], v[36:39]\n"
    "v_mfma_f32_16x16x32_bf16 v[68:71], a[212:215], a[148:151], v[68:71]\n"
    "s_add_u32 m0, s40, s64\n"
    "ds_read_b64_tr_b16 a[208:209], v3 offset:8448\n"
    "v_mfma_f32_16x16x32_bf16 v[100:103], a[212:215], a[152:155], v[100:103]\n"
    "ds_read_b64_tr_b16 a[210:211], v3 offset:8960\n"
    "v_mfma_f32_16x16x32_bf16 v[132:135], a[212:215], a[156:159], v[132:135]\n"
    "s_waitcnt lgkmcnt(8)\n"
    "v_mfma_f32_16x16x32_bf16 v[40:43], a[216:219], a[144:147], v[40:43]\n"
    "buffer_load_dwordx4 v10, s[20:23], s33 offen lds\n"
    "v_mfma_f32_16x16x32_bf16 v[72:75], a[216:219], a[148:151], v[72:75]\n"
    "ds_read_b64_tr_b16 a[212:213], v3 offset:8480\n"
    "ds_read_b64_tr_b16 a[214:215], v3 offset:8992\n"
    "v_mfma_f32_16x16x32_bf16 v[104:107], a[216:219], a[152:155], v[104:107]\n"
    "buffer_load_dwordx4 a[196:199], v19, s[28:31], s46 offen offset:3072\n"
    "v_mfma_f32_16x16x32_bf16 v[136:139], a[216:219], a[156:159], v[136:139]\n"
    "s_waitcnt lgkmcnt(8)\n"
    "v_mfma_f32_16x16x32_bf16 v[44:47], a[220:223], a[144:147], v[44:47]\n"
    "v_mfma_f32_16x16x32_bf16 v[76:79], a[220:223], a[148:151], v[76:79]\n"
    "ds_read_b64_tr_b16 a[216:217], v3 offset:8512\n"
    "ds_read_b64_tr_b16 a[218:219], v3 offset:9024\n"
    "v_mfma_f32_16x16x32_bf16 v[108:111], a[220:223], a[152:155], v[108:111]\n"
    "v_mfma_f32_16x16x32_bf16 v[140:143], a[220:223], a[156:159], v[140:143]\n"
    "s_waitcnt lgkmcnt(8)\n"
    "v_mfma_f32_16x16x32_bf16 v[48:51], a[224:227], a[144:147], v[48:51]\n"
    "v_mfma_f32_16x16x32_bf16 v[80:83], a[224:227], a[148:151], v[80:83]\n"
    "ds_read_b64_tr_b16 a[220:221], v3 offset:8544\n"
    "ds_read_b64_tr_b16 a[222:223], v3 offset:9056\n"
    "v_mfma_f32_16x16x32_bf16 v[112:115], a[224:227], a[152:155], v[112:115]\n"
    "s_add_u32 m0, s40, s65\n"
    "buffer_load_dwordx4 a[200:203], v19, s[28:31], s47 offen offset:3072\n"
    "v_mfma_f32_16x16x32_bf16 v[144:147], a[224:227], a[156:159], v[144:147]\n"
    "s_waitcnt lgkmcnt(8)\n"
    "v_mfma_f32_16x16x32_bf16 v[52:55], a[228:231], a[144:147], v[52:55]\n"
    "buffer_load_dwordx4 v11, s[20:23], s33 offen lds\n"
    "v_mfma_f32_16x16x32_bf16 v[84:87], a[228:231], a[148:151], v[84:87]\n"
    "ds_read_b64_tr_b16 a[224:225], v3 offset:12672\n"
    "ds_read_b64_tr_b16 a[226:227], v3 offset:13184\n"
    "v_mfma_f32_16x16x32_bf16 v[116:119], a[228:231], a[152:155], v[116:119]\n"
    "v_mfma_f32_16x16x32_bf16 v[148:151], a[228:231], a[156:159], v[148:151]\n"
    "s_waitcnt lgkmcnt(8)\n"
    "v_mfma_f32_16x16x32_bf16 v[56:59], a[232:235], a[144:147], v[56:59]\n"
    "v_mfma_f32_16x16x32_bf16 v[88:91], a[232:235], a[148:151], v[88:91]\n"
    "ds_read_b64_tr_b16 a[228:229], v3 offset:12704\n"
    "ds_read_b64_tr_b16 a[230:231], v3 offset:13216\n"
    "v_mfma_f32_16x16x32_bf16 v[120:123], a[232:235], a[152:155], v[120:123]\n"
    "buffer_load_dwordx4 a[204:207], v19, s[28:31], s48 offen offset:3072\n"
    "v_mfma_f32_16x16x32_bf16 v[152:155], a[232:235], a[156:159], v[152:155]\n"
    "s_waitcnt lgkmcnt(8)\n"
    "v_mfma_f32_16x16x32_bf16 v[60:63], a[236:239], a[144:147], v[60:63]\n"
    "v_mfma_f32_16x16x32_bf16 v[92:95], a[236:239], a[148:151], v[92:95]\n"
    "ds_read_b64_tr_b16 a[232:233], v3 offset:12736\n"
    "ds_read_b64_tr_b16 a[234:235], v3 offset:13248\n"
    "v_mfma_f32_16x16x32_bf16 v[124:127], a[236:239], a[152:155], v[124:127]\n"
    "v_mov_b32 v19, v4\n"
    "v_mfma_f32_16x16x32_bf16 v[156:159], a[236:239], a[156:159], v[156:159]\n"
    "s_waitcnt lgkmcnt(8) vmcnt(11)\n"
    "s_add_u32 m0, s40, s66\n"
    "v_mfma_f32_16x16x32_bf16 v[32:35], a[208:211], a[160:163], v[32:35]\n"
    "buffer_load_dwordx4 v12, s[20:23], s33 offen lds\n"
    "v_mfma_f32_16x16x32_bf16 v[64:67], a[208:211], a[164:167], v[64:67]\n"
    "ds_read_b64_tr_b16 a[236:237], v3 offset:12768\n"
    "ds_read_b64_tr_b16 a[238:239], v3 offset:13280\n"
    "v_mfma_f32_16x16x32_bf16 v[96:99], a[208:211], a[168:171], v[96:99]\n"
    "buffer_load_dwordx4 a[144:147], v4, s[28:31], 0 offen offset:0\n"
    "v_mfma_f32_16x16x32_bf16 v[128:131], a[208:211], a[172:175], v[128:131]\n"
    "s_waitcnt lgkmcnt(8)\n"
    "v_mfma_f32_16x16x32_bf16 v[36:39], a[212:215], a[160:163], v[36:39]\n"
    "v_mfma_f32_16x16x32_bf16 v[68:71], a[212:215], a[164:167], v[68:71]\n"
    "ds_read_b64_tr_b16 a[208:209], v3 offset:16896\n"
    "ds_read_b64_tr_b16 a[210:211], v3 offset:17408\n"
    "v_mfma_f32_16x16x32_bf16 v[100:103], a[212:215], a[168:171], v[100:103]\n"
    "v_mfma_f32_16x16x32_bf16 v[132:135], a[212:215], a[172:175], v[132:135]\n"
    "s_waitcnt lgkmcnt(8)\n"
    "v_mfma_f32_16x16x32_bf16 v[40:43], a[216:219], a[160:163], v[40:43]\n"
    "s_add_u32 s49, s49, 4096\n"
    "v_mfma_f32_16x16x32_bf16 v[72:75], a[216:219], a[164:167], v[72:75]\n"
    "ds_read_b64_tr_b16 a[212:213], v3 offset:16928\n"
    "ds_read_b64_tr_b16 a[214:215], v3 offset:17440\n"
    "v_mfma_f32_16x16x32_bf16 v[104:107], a[216:219], a[168:171], v[104:107]\n"
    "buffer_load_dwordx4 a[148:151], v4, s[28:31], s46 offen offset:0\n"
    "v_mfma_f32_16x16x32_bf16 v[136:139], a[216:219], a[172:175], v[136:139]\n"
    "s_waitcnt lgkmcnt(8)\n"
    "v_mfma_f32_16x16x32_bf16 v[44:47], a[220:223], a[160:163], v[44:47]\n"
    "s_add_u32 m0, s40, s67\n"
    "v_mfma_f32_16x16x32_bf16 v[76:79], a[220:223], a[164:167], v[76:79]\n"
    "ds_read_b64_tr_b16 a[216:217], v3 offset:16960\n"
    "ds_read_b64_tr_b16 a[218:219], v3 offset:17472\n"
    "v_mfma_f32_16x16x32_bf16 v[108:111], a[220:223], a[168:171], v[108:111]\n"
    "buffer_load_dwordx4 v13, s[20:23], s33 offen lds\n"
    "v_mfma_f32_16x16x32_bf16 v[140:143], a[220:223], a[172:175], v[140:143]\n"
    "s_waitcnt lgkmcnt(8)\n"
    "v_mfma_f32_16x16x32_bf16 v[48:51], a[224:227], a[160:163], v[48:51]\n"
    "s_cmp_eq_u32 s49, 20480\n"
    "s_cselect_b32 s49, 0, s49\n"
    "v_mfma_f32_16x16x32_bf16 v[80:83], a[224:227], a[164:167], v[80:83]\n"
    "ds_read_b64_tr_b16 a[220:221], v3 offset:16992\n"
    "ds_read_b64_tr_b16 a[222:223], v3 offset:17504\n"
    "v_mfma_f32_16x16x32_bf16 v[112:115], a[224:227], a[168:171], v[112:115]\n"
    "buffer_load_dwordx4 a[152:155], v4, s[28:31], s47 offen offset:0\n"
    "v_mfma_f32_16x16x32_bf16 v[144:147], a[224:227], a[172:175], v[144:147]\n"
    "s_waitcnt lgkmcnt(8)\n"
    "v_mfma_f32_16x16x32_bf16 v[52:55], a[228:231], a[160:163], v[52:55]\n"
    "v_mfma_f32_16x16x32_bf16 v[84:87], a[228:231], a[164:167], v[84:87]\n"
    "ds_read_b64_tr_b16 a[224:225], v3 offset:21120\n"
    "ds_read_b64_tr_b16 a[226:227], v3 offset:21632\n"
    "v_mfma_f32_16x16x32_bf16 v[116:119], a[228:231], a[168:171], v[116:119]\n"
    "v_mfma_f32_16x16x32_bf16 v[148:151], a[228:231], a[172:175], v[148:151]\n"
    "s_waitcnt lgkmcnt(8)\n"
    "v_mfma_f32_16x16x32_bf16 v[56:59], a[232:235], a[160:163], v[56:59]\n"
    "v_mfma_f32_16x16x32_bf16 v[88:91], a[232:235], a[164:167], v[88:91]\n"
    "s_add_u32 m0, s40, s68\n"
    "ds_read_b64_tr_b16 a[228:229], v3 offset:21152\n"
    "v_mfma_f32_16x16x32_bf16 v[120:123], a[232:235], a[168:171], v[120:123]\n"
    "ds_read_b64_tr_b16 a[230:231], v3 offset:21664\n"
    "buffer_load_dwordx4 a[156:159], v4, s[28:31], s48 offen offset:0\n"
    "v_mfma_f32_16x16x32_bf16 v[152:155], a[232:235], a[172:175], v[152:155]\n"
    "s_waitcnt lgkmcnt(8)\n"
    "v_mfma_f32_16x16x32_bf16 v[60:63], a[236:239], a[160:163], v[60:63]\n"
    "buffer_load_dwordx4 v14, s[24:27], s33 offen lds\n"
    "v_mfma_f32_16x16x32_bf16 v[92:95], a[236:239], a[164:167], v[92:95]\n"
    "ds_read_b64_tr_b16 a[232:233], v3 offset:21184\n"
    "ds_read_b64_tr_b16 a[234:235], v3 offset:21696\n"
    "v_mfma_f32_16x16x32_bf16 v[124:127], a[236:239], a[168:171], v[124:127]\n"
    "v_mfma_f32_16x16x32_bf16 v[156:159], a[236:239], a[172:175], v[156:159]\n"
    "s_waitcnt vmcnt(13) lgkmcnt(8)\n"
    "v_mfma_f32_16x16x32_bf16 v[32:35], a[208:211], a[176:179], v[32:35]\n"
    "v_mfma_f32_16x16x32_bf16 v[64:67], a[208:211], a[180:183], v[64:67]\n"
    "ds_read_b64_tr_b16 a[236:237], v3 offset:21216\n"
    "ds_read_b64_tr_b16 a[238:239], v3 offset:21728\n"
    "v_mfma_f32_16x16x32_bf16 v[96:99], a[208:211], a[184:187], v[96:99]\n"
    "buffer_load_dwordx4 a[160:163], v4, s[28:31], 0 offen offset:1024\n"
    "v_mfma_f32_16x16x32_bf16 v[128:131], a[208:211], a[188:191], v[128:131]\n"
    "s_waitcnt lgkmcnt(8)\n"
    "v_mfma_f32_16x16x32_bf16 v[36:39], a[212:215], a[176:179], v[36:39]\n"
    "v_mfma_f32_16x16x32_bf16 v[68:71], a[212:215], a[180:183], v[68:71]\n"
    "ds_read_b64_tr_b16 a[208:209], v3 offset:25344\n"
    "ds_read_b64_tr_b16 a[210:211], v3 offset:25856\n"
    "v_mfma_f32_16x16x32_bf16 v[100:103], a[212:215], a[184:187], v[100:103]\n"
    "s_add_u32 m0, s40, s69\n"
    "v_mfma_f32_16x16x32_bf16 v[132:135], a[212:215], a[188:191], v[132:135]\n"
    "s_waitcnt lgkmcnt(8)\n"
    "v_mfma_f32_16x16x32_bf16 v[40:43], a[216:219], a[176:179], v[40:43]\n"
    "buffer_load_dwordx4 v15, s[24:27], s33 offen lds\n"
    "v_mfma_f32_16x16x32_bf16 v[72:75], a[216:219], a[180:183], v[72:75]\n"
    "ds_read_b64_tr_b16 a[212:213], v3 offset:25376\n"
    "ds_read_b64_tr_b16 a[214:215], v3 offset:25888\n"
    "v_mfma_f32_16x16x32_bf16 v[104:107], a[216:219], a[184:187], v[104:107]\n"
    "buffer_load_dwordx4 a[164:167], v4, s[28:31], s46 offen offset:1024\n"
    "v_mfma_f32_16x16x32_bf16 v[136:139], a[216:219], a[188:191], v[136:139]\n"
    "s_waitcnt lgkmcnt(8)\n"
    "v_mfma_f32_16x16x32_bf16 v[44:47], a[220:223], a[176:179], v[44:47]\n"
    "v_mfma_f32_16x16x32_bf16 v[76:79], a[220:223], a[180:183], v[76:79]\n"
    "ds_read_b64_tr_b16 a[216:217], v3 offset:25408\n"
    "ds_read_b64_tr_b16 a[218:219], v3 offset:25920\n"
    "v_mfma_f32_16x16x32_bf16 v[108:111], a[220:223], a[184:187], v[108:111]\n"
    "v_mfma_f32_16x16x32_bf16 v[140:143], a[220:223], a[188:191], v[140:143]\n"
    "s_waitcnt lgkmcnt(8)\n"
    "v_mfma_f32_16x16x32_bf16 v[48:51], a[224:227], a[176:179], v[48:51]\n"
    "v_mfma_f32_16x16x32_bf16 v[80:83], a[224:227], a[180:183], v[80:83]\n"
    "ds_read_b64_tr_b16 a[220:221], v3 offset:25440\n"
    "ds_read_b64_tr_b16 a[222:223], v3 offset:25952\n"
    "v_mfma_f32_16x16x32_bf16 v[112:115], a[224:227], a[184:187], v[112:115]\n"
    "buffer_load_dwordx4 a[168:171], v4, s[28:31], s47 offen offset:1024\n"
    "v_mfma_f32_16x16x32_bf16 v[144:147], a[224:227], a[188:191], v[144:147]\n"
    "s_waitcnt lgkmcnt(8)\n"
    "s_add_u32 m0, s40, s70\n"
    "v_mfma_f32_16x16x32_bf16 v[52:55], a[228:231], a[176:179], v[52:55]\n"
    "buffer_load_dwordx4 v16, s[24:27], s33 offen lds\n"
    "v_mfma_f32_16x16x32_bf16 v[84:87], a[228:231], a[180:183], v[84:87]\n"
    "ds_read_b64_tr_b16 a[224:225], v3 offset:29568\n"
    "ds_read_b64_tr_b16 a[226:227], v3 offset:30080\n"
    "v_mfma_f32_16x16x32_bf16 v[116:119], a[228:231], a[184:187], v[116:119]\n"
    "v_mfma_f32_16x16x32_bf16 v[148:151], a[228:231], a[188:191], v[148:151]\n"
    "s_waitcnt lgkmcnt(8)\n"
    "v_mfma_f32_16x16x32_bf16 v[56:59], a[232:235], a[176:179], v[56:59]\n"
    "v_mfma_f32_16x16x32_bf16 v[88:91], a[232:235], a[180:183], v[88:91]\n"
    "ds_read_b64_tr_b16 a[228:229], v3 offset:29600\n"
    "ds_read_b64_tr_b16 a[230:231], v3 offset:30112\n"
    "v_mfma_f32_16x16x32_bf16 v[120:123], a[232:235], a[184:187], v[120:123]\n"
    "buffer_load_dwordx4 a[172:175], v4, s[28:31], s48 offen offset:1024\n"
    "v_mfma_f32_16x16x32_bf16 v[152:155], a[232:235], a[188:191], v[152:155]\n"
    "v_mfma_f32_16x16x32_bf16 v[60:63], a[236:239], a[176:179], v[60:63]\n"
    "v_mfma_f32_16x16x32_bf16 v[92:95], a[236:239], a[180:183], v[92:95]\n"
    "ds_read_b64_tr_b16 a[232:233], v3 offset:29632\n"
    "ds_read_b64_tr_b16 a[234:235], v3 offset:30144\n"
    "v_mfma_f32_16x16x32_bf16 v[124:127], a[236:239], a[184:187], v[124:127]\n"
    "v_mfma_f32_16x16x32_bf16 v[156:159], a[236:239], a[188:191], v[156:159]\n"
    "s_waitcnt vmcnt(13) lgkmcnt(8)\n"
    "v_mfma_f32_16x16x32_bf16 v[32:35], a[208:211], a[192:195], v[32:35]\n"
    "s_add_u32 m0, s40, s71\n"
    "v_mfma_f32_16x16x32_bf16 v[64:67], a[208:211], a[196:199], v[64:67]\n"
    "ds_read_b64_tr_b16 a[236:237], v3 offset:29664\n"
    "ds_read_b64_tr_b16 a[238:239], v3 offset:30176\n"
    "v_mfma_f32_16x16x32_bf16 v[96:99], a[208:211], a[200:203], v[96:99]\n"
    "buffer_load_dwordx4 v17, s[24:27], s33 offen lds\n"
    "v_mfma_f32_16x16x32_bf16 v[128:131], a[208:211], a[204:207], v[128:131]\n"
    "s_barrier\n"
    "buffer_load_dwordx4 a[176:179], v4, s[28:31], 0 offen offset:2048\n"
    "v_mfma_f32_16x16x32_bf16 v[36:39], a[212:215], a[192:195], v[36:39]\n"
    "s_add_u32 s33, s33, s41\n"
    "s_add_u32 s40, s40, s51\n"
    "v_mfma_f32_16x16x32_bf16 v[68:71], a[212:215], a[196:199], v[68:71]\n"
    "v_add_u32 v3, s44, v3\n"
    "ds_read_b64_tr_b16 a[208:209], v3 offset:0\n"
    "v_mfma_f32_16x16x32_bf16 v[100:103], a[212:215], a[200:203], v[100:103]\n"
    "ds_read_b64_tr_b16 a[210:211], v3 offset:512\n"
    "v_mfma_f32_16x16x32_bf16 v[132:135], a[212:215], a[204:207], v[132:135]\n"
    "s_waitcnt lgkmcnt(8)\n"
    "v_mfma_f32_16x16x32_bf16 v[40:43], a[216:219], a[192:195], v[40:43]\n"
    "s_add_u32 s43, s43, s44\n"
    "buffer_load_dwordx4 a[180:183], v4, s[28:31], s46 offen offset:2048\n"
    "v_mfma_f32_16x16x32_bf16 v[72:75], a[216:219], a[196:199], v[72:75]\n"
    "ds_read_b64_tr_b16 a[212:213], v3 offset:32\n"
    "ds_read_b64_tr_b16 a[214:215], v3 offset:544\n"
    "v_mfma_f32_16x16x32_bf16 v[104:107], a[216:219], a[200:203], v[104:107]\n"
    "s_cmp_eq_u32 s43, 101376\n"
    "s_cselect_b32 s44, -101376, s51\n"
    "v_mfma_f32_16x16x32_bf16 v[136:139], a[216:219], a[204:207], v[136:139]\n"
    "s_waitcnt lgkmcnt(8)\n"
    "v_mfma_f32_16x16x32_bf16 v[44:47], a[220:223], a[192:195], v[44:47]\n"
    "s_cmp_eq_u32 s40, 135168\n"
    "s_cselect_b32 s40, 0, s40\n"
    "v_mfma_f32_16x16x32_bf16 v[76:79], a[220:223], a[196:199], v[76:79]\n"
    "ds_read_b64_tr_b16 a[216:217], v3 offset:64\n"
    "ds_read_b64_tr_b16 a[218:219], v3 offset:576\n"
    "v_mfma_f32_16x16x32_bf16 v[108:111], a[220:223], a[200:203], v[108:111]\n"
    "buffer_load_dwordx4 a[184:187], v4, s[28:31], s47 offen offset:2048\n"
    "v_mfma_f32_16x16x32_bf16 v[140:143], a[220:223], a[204:207], v[140:143]\n"
    "s_waitcnt lgkmcnt(8)\n"
    "v_mfma_f32_16x16x32_bf16 v[48:51], a[224:227], a[192:195], v[48:51]\n"
    "v_mfma_f32_16x16x32_bf16 v[80:83], a[224:227], a[196:199], v[80:83]\n"
    "ds_read_b64_tr_b16 a[220:221], v3 offset:96\n"
    "ds_read_b64_tr_b16 a[222:223], v3 offset:608\n"
    "v_mfma_f32_16x16x32_bf16 v[112:115], a[224:227], a[200:203], v[112:115]\n"
    "v_mfma_f32_16x16x32_bf16 v[144:147], a[224:227], a[204:207], v[144:147]\n"
    "s_waitcnt lgkmcnt(8)\n"
    "v_mfma_f32_16x16x32_bf16 v[52:55], a[228:231], a[192:195], v[52:55]\n"
    "buffer_load_dwordx4 a[188:191], v4, s[28:31], s48 offen offset:2048\n"
    "v_mfma_f32_16x16x32_bf16 v[84:87], a[228:231], a[196:199], v[84:87]\n"
    "ds_read_b64_tr_b16 a[224:225], v3 offset:4224\n"
    "ds_read_b64_tr_b16 a[226:227], v3 offset:4736\n"
    "v_mfma_f32_16x16x32_bf16 v[116:119], a[228:231], a[200:203], v[116:119]\n"
    "v_add_u32 v4, s49, v2\n"
    "v_mfma_f32_16x16x32_bf16 v[148:151], a[228:231], a[204:207], v[148:151]\n"
    "v_mfma_f32_16x16x32_bf16 v[56:59], a[232:235], a[192:195], v[56:59]\n"
    "v_mfma_f32_16x16x32_bf16 v[88:91], a[232:235], a[196:199], v[88:91]\n"
    "ds_read_b64_tr_b16 a[228:229], v3 offset:4256\n"
    "ds_read_b64_tr_b16 a[230:231], v3 offset:4768\n"
    "v_mfma_f32_16x16x32_bf16 v[120:123], a[232:235], a[200:203], v[120:123]\n"
    "v_mfma_f32_16x16x32_bf16 v[152:155], a[232:235], a[204:207], v[152:155]\n"
    "s_waitcnt lgkmcnt(8)\n"
    "v_mfma_f32_16x16x32_bf16 v[60:63], a[236:239], a[192:195], v[60:63]\n"
    "v_mfma_f32_16x16x32_bf16 v[92:95], a[236:239], a[196:199], v[92:95]\n"
    "v_mfma_f32_16x16x32_bf16 v[124:127], a[236:239], a[200:203], v[124:127]\n"
    "v_mfma_f32_16x16x32_bf16 v[156:159], a[236:239], a[204:207], v[156:159]\n"
    "s_cmp_lg_u32 s50, 0\n"
    "s_cbranch_scc1 K1_F\n"
    "s_mov_b32 s52, 0xbfb8aa3b\n"
    "s_mov_b32 s53, s52\n"
    "s_mov_b32 s54, 1.0\n"
    "s_mov_b32 s55, s54\n"
    "s_lshl_b32 s56, s74, 11\n"
    "s_add_u32 s56, s56, 131072\n"
    "s_add_u32 s57, s56, 0\n"
    "v_pk_mul_f32 v[32:33], v[32:33], s[52:53]\n"
    "v_pk_mul_f32 v[34:35], v[34:35], s[52:53]\n"
    "v_pk_mul_f32 v[36:37], v[36:37], s[52:53]\n"
    "v_pk_mul_f32 v[38:39], v[38:39], s[52:53]\n"
    "v_pk_mul_f32 v[40:41], v[40:41], s[52:53]\n"
    "v_pk_mul_f32 v[42:43], v[42:43], s[52:53]\n"
    "v_pk_mul_f32 v[44:45], v[44:45], s[52:53]\n"
    "v_pk_mul_f32 v[46:47], v[46:47], s[52:53]\n"
    "v_exp_f32 v32, v32\n"
    "v_exp_f32 v33, v33\n"
    "v_exp_f32 v34, v34\n"
    "v_exp_f32 v35, v35\n"
    "v_exp_f32 v36, v36\n"
    "v_exp_f32 v37, v37\n"
    "v_exp_f32 v38, v38\n"
    "v_exp_f32 v39, v39\n"
    "v_exp_f32 v40, v40\n"
    "v_exp_f32 v41, v41\n"
    "v_exp_f32 v42, v42\n"
    "v_exp_f32 v43, v43\n"
    "v_exp_f32 v44, v44\n"
    "v_exp_f32 v45, v45\n"
    "v_exp_f32 v46, v46\n"
    "v_exp_f32 v47, v47\n"
    "v_pk_add_f32 v[32:33], v[32:33], s[54:55]\n"
    "v_pk_add_f32 v[34:35], v[34:35], s[54:55]\n"
    "v_pk_add_f32 v[36:37], v[36:37], s[54:55]\n"
    "v_pk_add_f32 v[38:39], v[38:39], s[54:55]\n"
    "v_pk_add_f32 v[40:41], v[40:41], s[54:55]\n"
    "v_pk_add_f32 v[42:43], v[42:43], s[54:55]\n"
    "v_pk_add_f32 v[44:45], v[44:45], s[54:55]\n"
    "v_pk_add_f32 v[46:47], v[46:47], s[54:55]\n"
    "v_rcp_f32 v32, v32\n"
    "v_rcp_f32 v33, v33\n"
    "v_rcp_f32 v34, v34\n"
    "v_rcp_f32 v35, v35\n"
    "v_rcp_f32 v36, v36\n"
    "v_rcp_f32 v37, v37\n"
    "v_rcp_f32 v38, v38\n"
    "v_rcp_f32 v39, v39\n"
    "v_rcp_f32 v40, v40\n"
    "v_rcp_f32 v41, v41\n"
    "v_rcp_f32 v42, v42\n"
    "v_rcp_f32 v43, v43\n"
    "v_rcp_f32 v44, v44\n"
    "v_rcp_f32 v45, v45\n"
    "v_rcp_f32 v46, v46\n"
    "v_rcp_f32 v47, v47\n"
    "v_pk_mul_f32 v[32:33], v[32:33], v[48:49]\n"
    "v_pk_mul_f32 v[34:35], v[34:35], v[50:51]\n"
    "v_pk_mul_f32 v[36:37], v[36:37], v[52:53]\n"
    "v_pk_mul_f32 v[38:39], v[38:39], v[54:55]\n"
    "v_pk_mul_f32 v[40:41], v[40:41], v[56:57]\n"
    "v_pk_mul_f32 v[42:43], v[42:43], v[58:59]\n"
    "v_pk_mul_f32 v[44:45], v[44:45], v[60:61]\n"
    "v_pk_mul_f32 v[46:47], v[46:47], v[62:63]\n"
    "v_cvt_pk_bf16_f32 v32, v32, v33\n"
    "v_cvt_pk_bf16_f32 v33, v34, v35\n"
    "v_cvt_pk_bf16_f32 v34, v36, v37\n"
    "v_cvt_pk_bf16_f32 v35, v38, v39\n"
    "v_cvt_pk_bf16_f32 v36, v40, v41\n"
    "v_cvt_pk_bf16_f32 v37, v42, v43\n"
    "v_cvt_pk_bf16_f32 v38, v44, v45\n"
    "v_cvt_pk_bf16_f32 v39, v46, v47\n"
    "buffer_store_dwordx4 v[32:35], v2, s[36:39], s57 offen offset:0 sc1\n"
    "buffer_store_dwordx4 v[36:39], v2, s[36:39], s57 offen offset:1024 sc1\n"
    "s_add_u32 s57, s56, 188416\n"
    "v_pk_mul_f32 v[64:65], v[64:65], s[52:53]\n"
    "v_pk_mul_f32 v[66:67], v[66:67], s[52:53]\n"
    "v_pk_mul_f32 v[68:69], v[68:69], s[52:53]\n"
    "v_pk_mul_f32 v[70:71], v[70:71], s[52:53]\n"
    "v_pk_mul_f32 v[72:73], v[72:73], s[52:53]\n"
    "v_pk_mul_f32 v[74:75], v[74:75], s[52:53]\n"
    "v_pk_mul_f32 v[76:77], v[76:77], s[52:53]\n"
    "v_pk_mul_f32 v[78:79], v[78:79], s[52:53]\n"
    "v_exp_f32 v64, v64\n"
    "v_exp_f32 v65, v65\n"
    "v_exp_f32 v66, v66\n"
    "v_exp_f32 v67, v67\n"
    "v_exp_f32 v68, v68\n"
    "v_exp_f32 v69, v69\n"
    "v_exp_f32 v70, v70\n"
    "v_exp_f32 v71, v71\n"
    "v_exp_f32 v72, v72\n"
    "v_exp_f32 v73, v73\n"
    "v_exp_f32 v74, v74\n"
    "v_exp_f32 v75, v75\n"
    "v_exp_f32 v76, v76\n"
    "v_exp_f32 v77, v77\n"
    "v_exp_f32 v78, v78\n"
    "v_exp_f32 v79, v79\n"
    "v_pk_add_f32 v[64:65], v[64:65], s[54:55]\n"
    "v_pk_add_f32 v[66:67], v[66:67], s[54:55]\n"
    "v_pk_add_f32 v[68:69], v[68:69], s[54:55]\n"
    "v_pk_add_f32 v[70:71], v[70:71], s[54:55]\n"
    "v_pk_add_f32 v[72:73], v[72:73], s[54:55]\n"
    "v_pk_add_f32 v[74:75], v[74:75], s[54:55]\n"
    "v_pk_add_f32 v[76:77], v[76:77], s[54:55]\n"
    "v_pk_add_f32 v[78:79], v[78:79], s[54:55]\n"
    "v_rcp_f32 v64, v64\n"
    "v_rcp_f32 v65, v65\n"
    "v_rcp_f32 v66, v66\n"
    "v_rcp_f32 v67, v67\n"
    "v_rcp_f32 v68, v68\n"
    "v_rcp_f32 v69, v69\n"
    "v_rcp_f32 v70, v70\n"
    "v_rcp_f32 v71, v71\n"
    "v_rcp_f32 v72, v72\n"
    "v_rcp_f32 v73, v73\n"
    "v_rcp_f32 v74, v74\n"
    "v_rcp_f32 v75, v75\n"
    "v_rcp_f32 v76, v76\n"
    "v_rcp_f32 v77, v77\n"
    "v_rcp_f32 v78, v78\n"
    "v_rcp_f32 v79, v79\n"
    "v_pk_mul_f32 v[64:65], v[64:65], v[80:81]\n"
    "v_pk_mul_f32 v[66:67], v[66:67], v[82:83]\n"
    "v_pk_mul_f32 v[68:69], v[68:69], v[84:85]\n"
    "v_pk_mul_f32 v[70:71], v[70:71], v[86:87]\n"
    "v_pk_mul_f32 v[72:73], v[72:73], v[88:89]\n"
    "v_pk_mul_f32 v[74:75], v[74:75], v[90:91]\n"
    "v_pk_mul_f32 v[76:77], v[76:77], v[92:93]\n"
    "v_pk_mul_f32 v[78:79], v[78:79], v[94:95]\n"
    "v_cvt_pk_bf16_f32 v64, v64, v65\n"
    "v_cvt_pk_bf16_f32 v65, v66, v67\n"
    "v_cvt_pk_bf16_f32 v66, v68, v69\n"
    "v_cvt_pk_bf16_f32 v67, v70, v71\n"
    "v_cvt_pk_bf16_f32 v68, v72, v73\n"
    "v_cvt_pk_bf16_f32 v69, v74, v75\n"
    "v_cvt_pk_bf16_f32 v70, v76, v77\n"
    "v_cvt_pk_bf16_f32 v71, v78, v79\n"
    "buffer_store_dwordx4 v[64:67], v2, s[36:39], s57 offen offset:0 sc1\n"
    "buffer_store_dwordx4 v[68:71], v2, s[36:39], s57 offen offset:1024 sc1\n"
    "s_add_u32 s57, s56, 376832\n"
    "v_pk_mul_f32 v[96:97], v[96:97], s[52:53]\n"
    "v_pk_mul_f32 v[98:99], v[98:99], s[52:53]\n"
    "v_pk_mul_f32 v[100:101], v[100:101], s[52:53]\n"
    "v_pk_mul_f32 v[102:103], v[102:103], s[52:53]\n"
    "v_pk_mul_f32 v[104:105], v[104:105], s[52:53]\n"
    "v_pk_mul_f32 v[106:107], v[106:107], s[52:53]\n"
    "v_pk_mul_f32 v[108:109], v[108:109], s[52:53]\n"
    "v_pk_mul_f32 v[110:111], v[110:111], s[52:53]\n"
    "v_exp_f32 v96, v96\n"
    "v_exp_f32 v97, v97\n"
    "v_exp_f32 v98, v98\n"
    "v_exp_f32 v99, v99\n"
    "v_exp_f32 v100, v100\n"
    "v_exp_f32 v101, v101\n"
    "v_exp_f32 v102, v102\n"
    "v_exp_f32 v103, v103\n"
    "v_exp_f32 v104, v104\n"
    "v_exp_f32 v105, v105\n"
    "v_exp_f32 v106, v106\n"
    "v_exp_f32 v107, v107\n"
    "v_exp_f32 v108, v108\n"
    "v_exp_f32 v109, v109\n"
    "v_exp_f32 v110, v110\n"
    "v_exp_f32 v111, v111\n"
    "v_pk_add_f32 v[96:97], v[96:97], s[54:55]\n"
    "v_pk_add_f32 v[98:99], v[98:99], s[54:55]\n"
    "v_pk_add_f32 v[100:101], v[100:101], s[54:55]\n"
    "v_pk_add_f32 v[102:103], v[102:103], s[54:55]\n"
    "v_pk_add_f32 v[104:105], v[104:105], s[54:55]\n"
    "v_pk_add_f32 v[106:107], v[106:107], s[54:55]\n"
    "v_pk_add_f32 v[108:109], v[108:109], s[54:55]\n"
    "v_pk_add_f32 v[110:111], v[110:111], s[54:55]\n"
    "v_rcp_f32 v96, v96\n"
    "v_rcp_f32 v97, v97\n"
    "v_rcp_f32 v98, v98\n"
    "v_rcp_f32 v99, v99\n"
    "v_rcp_f32 v100, v100\n"
    "v_rcp_f32 v101, v101\n"
    "v_rcp_f32 v102, v102\n"
    "v_rcp_f32 v103, v103\n"
    "v_rcp_f32 v104, v104\n"
    "v_rcp_f32 v105, v105\n"
    "v_rcp_f32 v106, v106\n"
    "v_rcp_f32 v107, v107\n"
    "v_rcp_f32 v108, v108\n"
    "v_rcp_f32 v109, v109\n"
    "v_rcp_f32 v110, v110\n"
    "v_rcp_f32 v111, v111\n"
    "v_pk_mul_f32 v[96:97], v[96:97], v[112:113]\n"
    "v_pk_mul_f32 v[98:99], v[98:99], v[114:115]\n"
    "v_pk_mul_f32 v[100:101], v[100:101], v[116:117]\n"
    "v_pk_mul_f32 v[102:103], v[102:103], v[118:119]\n"
    "v_pk_mul_f32 v[104:105], v[104:105], v[120:121]\n"
    "v_pk_mul_f32 v[106:107], v[106:107], v[122:123]\n"
    "v_pk_mul_f32 v[108:109], v[108:109], v[124:125]\n"
    "v_pk_mul_f32 v[110:111], v[110:111], v[126:127]\n"
    "v_cvt_pk_bf16_f32 v96, v96, v97\n"
    "v_cvt_pk_bf16_f32 v97, v98, v99\n"
    "v_cvt_pk_bf16_f32 v98, v100, v101\n"
    "v_cvt_pk_bf16_f32 v99, v102, v103\n"
    "v_cvt_pk_bf16_f32 v100, v104, v105\n"
    "v_cvt_pk_bf16_f32 v101, v106, v107\n"
    "v_cvt_pk_bf16_f32 v102, v108, v109\n"
    "v_cvt_pk_bf16_f32 v103, v110, v111\n"
    "buffer_store_dwordx4 v[96:99], v2, s[36:39], s57 offen offset:0 sc1\n"
    "buffer_store_dwordx4 v[100:103], v2, s[36:39], s57 offen offset:1024 sc1\n"
    "s_add_u32 s57, s56, 565248\n"
    "v_pk_mul_f32 v[128:129], v[128:129], s[52:53]\n"
    "v_pk_mul_f32 v[130:131], v[130:131], s[52:53]\n"
    "v_pk_mul_f32 v[132:133], v[132:133], s[52:53]\n"
    "v_pk_mul_f32 v[134:135], v[134:135], s[52:53]\n"
    "v_pk_mul_f32 v[136:137], v[136:137], s[52:53]\n"
    "v_pk_mul_f32 v[138:139], v[138:139], s[52:53]\n"
    "v_pk_mul_f32 v[140:141], v[140:141], s[52:53]\n"
    "v_pk_mul_f32 v[142:143], v[142:143], s[52:53]\n"
    "v_exp_f32 v128, v128\n"
    "v_exp_f32 v129, v129\n"
    "v_exp_f32 v130, v130\n"
    "v_exp_f32 v131, v131\n"
    "v_exp_f32 v132, v132\n"
    "v_exp_f32 v133, v133\n"
    "v_exp_f32 v134, v134\n"
    "v_exp_f32 v135, v135\n"
    "v_exp_f32 v136, v136\n"
    "v_exp_f32 v137, v137\n"
    "v_exp_f32 v138, v138\n"
    "v_exp_f32 v139, v139\n"
    "v_exp_f32 v140, v140\n"
    "v_exp_f32 v141, v141\n"
    "v_exp_f32 v142, v142\n"
    "v_exp_f32 v143, v143\n"
    "v_pk_add_f32 v[128:129], v[128:129], s[54:55]\n"
    "v_pk_add_f32 v[130:131], v[130:131], s[54:55]\n"
    "v_pk_add_f32 v[132:133], v[132:133], s[54:55]\n"
    "v_pk_add_f32 v[134:135], v[134:135], s[54:55]\n"
    "v_pk_add_f32 v[136:137], v[136:137], s[54:55]\n"
    "v_pk_add_f32 v[138:139], v[138:139], s[54:55]\n"
    "v_pk_add_f32 v[140:141], v[140:141], s[54:55]\n"
    "v_pk_add_f32 v[142:143], v[142:143], s[54:55]\n"
    "v_rcp_f32 v128, v128\n"
    "v_rcp_f32 v129, v129\n"
    "v_rcp_f32 v130, v130\n"
    "v_rcp_f32 v131, v131\n"
    "v_rcp_f32 v132, v132\n"
    "v_rcp_f32 v133, v133\n"
    "v_rcp_f32 v134, v134\n"
    "v_rcp_f32 v135, v135\n"
    "v_rcp_f32 v136, v136\n"
    "v_rcp_f32 v137, v137\n"
    "v_rcp_f32 v138, v138\n"
    "v_rcp_f32 v139, v139\n"
    "v_rcp_f32 v140, v140\n"
    "v_rcp_f32 v141, v141\n"
    "v_rcp_f32 v142, v142\n"
    "v_rcp_f32 v143, v143\n"
    "v_pk_mul_f32 v[128:129], v[128:129], v[144:145]\n"
    "v_pk_mul_f32 v[130:131], v[130:131], v[146:147]\n"
    "v_pk_mul_f32 v[132:133], v[132:133], v[148:149]\n"
    "v_pk_mul_f32 v[134:135], v[134:135], v[150:151]\n"
    "v_pk_mul_f32 v[136:137], v[136:137], v[152:153]\n"
    "v_pk_mul_f32 v[138:139], v[138:139], v[154:155]\n"
    "v_pk_mul_f32 v[140:141], v[140:141], v[156:157]\n"
    "v_pk_mul_f32 v[142:143], v[142:143], v[158:159]\n"
    "v_cvt_pk_bf16_f32 v128, v128, v129\n"
    "v_cvt_pk_bf16_f32 v129, v130, v131\n"
    "v_cvt_pk_bf16_f32 v130, v132, v133\n"
    "v_cvt_pk_bf16_f32 v131, v134, v135\n"
    "v_cvt_pk_bf16_f32 v132, v136, v137\n"
    "v_cvt_pk_bf16_f32 v133, v138, v139\n"
    "v_cvt_pk_bf16_f32 v134, v140, v141\n"
    "v_cvt_pk_bf16_f32 v135, v142, v143\n"
    "buffer_store_dwordx4 v[128:131], v2, s[36:39], s57 offen offset:0 sc1\n"
    "buffer_store_dwordx4 v[132:135], v2, s[36:39], s57 offen offset:1024 sc1\n"
    "s_branch K1_END\n"
    "K1_EX:\n"
    "s_waitcnt lgkmcnt(7)\n"
    "v_accvgpr_read_b32 v176, a0\n"
    "v_accvgpr_read_b32 v177, a1\n"
    "v_accvgpr_read_b32 v178, a2\n"
    "v_accvgpr_read_b32 v179, a3\n"
    "s_waitcnt lgkmcnt(6)\n"
    "v_accvgpr_read_b32 v180, a4\n"
    "v_accvgpr_read_b32 v181, a5\n"
    "v_accvgpr_read_b32 v182, a6\n"
    "v_accvgpr_read_b32 v183, a7\n"
    "v_pk_mul_f32 v[176:177], v[176:177], v[224:225] op_sel_hi:[1,0]\n"
    "v_pk_mul_f32 v[178:179], v[178:179], v[224:225] op_sel_hi:[1,0]\n"
    "v_pk_mul_f32 v[180:181], v[180:181], v[224:225] op_sel_hi:[1,0]\n"
    "v_pk_mul_f32 v[182:183], v[182:183], v[224:225] op_sel_hi:[1,0]\n"
    "s_waitcnt lgkmcnt(5)\n"
    "v_cvt_pk_bf16_f32 v184, v176, v177\n"
    "v_cvt_pk_bf16_f32 v185, v178, v179\n"
    "v_cvt_pk_bf16_f32 v186, v180, v181\n"
    "v_cvt_pk_bf16_f32 v187, v182, v183\n"
    "buffer_store_dwordx4 v[184:187], v2, s[36:39], s52 offen offset:0 sc1\n"
    "s_waitcnt lgkmcnt(4)\n"
    "v_accvgpr_read_b32 v188, a8\n"
    "v_accvgpr_read_b32 v189, a9\n"
    "v_accvgpr_read_b32 v190, a10\n"
    "v_accvgpr_read_b32 v191, a11\n"
    "s_waitcnt lgkmcnt(3)\n"
    "v_accvgpr_read_b32 v192, a12\n"
    "v_accvgpr_read_b32 v193, a13\n"
    "v_accvgpr_read_b32 v194, a14\n"
    "v_accvgpr_read_b32 v195, a15\n"
    "v_pk_mul_f32 v[188:189], v[188:189], v[224:225] op_sel_hi:[1,0]\n"
    "v_pk_mul_f32 v[190:191], v[190:191], v[224:225] op_sel_hi:[1,0]\n"
    "v_pk_mul_f32 v[192:193], v[192:193], v[224:225] op_sel_hi:[1,0]\n"
    "v_pk_mul_f32 v[194:195], v[194:195], v[224:225] op_sel_hi:[1,0]\n"
    "s_waitcnt lgkmcnt(2)\n"
    "v_cvt_pk_bf16_f32 v196, v188, v189\n"
    "v_cvt_pk_bf16_f32 v197, v190, v191\n"
    "v_cvt_pk_bf16_f32 v198, v192, v193\n"
    "v_cvt_pk_bf16_f32 v199, v194, v195\n"
    "buffer_store_dwordx4 v[196:199], v2, s[36:39], s52 offen offset:1024 sc1\n"
    "s_waitcnt lgkmcnt(1)\n"
    "v_accvgpr_read_b32 v200, a16\n"
    "v_accvgpr_read_b32 v201, a17\n"
    "v_accvgpr_read_b32 v202, a18\n"
    "v_accvgpr_read_b32 v203, a19\n"
    "s_waitcnt lgkmcnt(0)\n"
    "v_accvgpr_read_b32 v204, a20\n"
    "v_accvgpr_read_b32 v205, a21\n"
    "v_accvgpr_read_b32 v206, a22\n"
    "v_accvgpr_read_b32 v207, a23\n"
    "v_pk_mul_f32 v[200:201], v[200:201], v[224:225] op_sel_hi:[1,0]\n"
    "v_pk_mul_f32 v[202:203], v[202:203], v[224:225] op_sel_hi:[1,0]\n"
    "v_pk_mul_f32 v[204:205], v[204:205], v[224:225] op_sel_hi:[1,0]\n"
    "v_pk_mul_f32 v[206:207], v[206:207], v[224:225] op_sel_hi:[1,0]\n"
    "v_cvt_pk_bf16_f32 v208, v200, v201\n"
    "v_cvt_pk_bf16_f32 v209, v202, v203\n"
    "v_cvt_pk_bf16_f32 v210, v204, v205\n"
    "v_cvt_pk_bf16_f32 v211, v206, v207\n"
    "buffer_store_dwordx4 v[208:211], v2, s[36:39], s52 offen offset:2048 sc1\n"
    "v_accvgpr_read_b32 v212, a24\n"
    "v_accvgpr_read_b32 v213, a25\n"
    "v_accvgpr_read_b32 v214, a26\n"
    "v_accvgpr_read_b32 v215, a27\n"
    "v_accvgpr_read_b32 v216, a28\n"
    "v_accvgpr_read_b32 v217, a29\n"
    "v_accvgpr_read_b32 v218, a30\n"
    "v_accvgpr_read_b32 v219, a31\n"
    "v_pk_mul_f32 v[212:213], v[212:213], v[224:225] op_sel_hi:[1,0]\n"
    "v_pk_mul_f32 v[214:215], v[214:215], v[224:225] op_sel_hi:[1,0]\n"
    "v_pk_mul_f32 v[216:217], v[216:217], v[224:225] op_sel_hi:[1,0]\n"
    "v_pk_mul_f32 v[218:219], v[218:219], v[224:225] op_sel_hi:[1,0]\n"
    "v_cvt_pk_bf16_f32 v220, v212, v213\n"
    "v_cvt_pk_bf16_f32 v221, v214, v215\n"
    "v_cvt_pk_bf16_f32 v222, v216, v217\n"
    "v_cvt_pk_bf16_f32 v223, v218, v219\n"
    "buffer_store_dwordx4 v[220:223], v2, s[36:39], s52 offen offset:3072 sc1\n"
    "v_accvgpr_read_b32 v176, a32\n"
    "v_accvgpr_read_b32 v177, a33\n"
    "v_accvgpr_read_b32 v178, a34\n"
    "v_accvgpr_read_b32 v179, a35\n"
    "v_accvgpr_read_b32 v180, a36\n"
    "v_accvgpr_read_b32 v181, a37\n"
    "v_accvgpr_read_b32 v182, a38\n"
    "v_accvgpr_read_b32 v183, a39\n"
    "v_pk_mul_f32 v[176:177], v[176:177], v[226:227] op_sel_hi:[1,0]\n"
    "v_pk_mul_f32 v[178:179], v[178:179], v[226:227] op_sel_hi:[1,0]\n"
    "v_pk_mul_f32 v[180:181], v[180:181], v[226:227] op_sel_hi:[1,0]\n"
    "v_pk_mul_f32 v[182:183], v[182:183], v[226:227] op_sel_hi:[1,0]\n"
    "v_cvt_pk_bf16_f32 v184, v176, v177\n"
    "v_cvt_pk_bf16_f32 v185, v178, v179\n"
    "v_cvt_pk_bf16_f32 v186, v180, v181\n"
    "v_cvt_pk_bf16_f32 v187, v182, v183\n"
    "buffer_store_dwordx4 v[184:187], v2, s[36:39], s53 offen offset:0 sc1\n"
    "v_accvgpr_read_b32 v188, a40\n"
    "v_accvgpr_read_b32 v189, a41\n"
    "v_accvgpr_read_b32 v190, a42\n"
    "v_accvgpr_read_b32 v191, a43\n"
    "v_accvgpr_read_b32 v192, a44\n"
    "v_accvgpr_read_b32 v193, a45\n"
    "v_accvgpr_read_b32 v194, a46\n"
    "v_accvgpr_read_b32 v195, a47\n"
    "v_pk_mul_f32 v[188:189], v[188:189], v[226:227] op_sel_hi:[1,0]\n"
    "v_pk_mul_f32 v[190:191], v[190:191], v[226:227] op_sel_hi:[1,0]\n"
    "v_pk_mul_f32 v[192:193], v[192:193], v[226:227] op_sel_hi:[1,0]\n"
    "v_pk_mul_f32 v[194:195], v[194:195], v[226:227] op_sel_hi:[1,0]\n"
    "v_cvt_pk_bf16_f32 v196, v188, v189\n"
    "v_cvt_pk_bf16_f32 v197, v190, v191\n"
    "v_cvt_pk_bf16_f32 v198, v192, v193\n"
    "v_cvt_pk_bf16_f32 v199, v194, v195\n"
    "buffer_store_dwordx4 v[196:199], v2, s[36:39], s53 offen offset:1024 sc1\n"
    "v_accvgpr_read_b32 v200, a48\n"
    "v_accvgpr_read_b32 v201, a49\n"
    "v_accvgpr_read_b32 v202, a50\n"
    "v_accvgpr_read_b32 v203, a51\n"
    "v_accvgpr_read_b32 v204, a52\n"
    "v_accvgpr_read_b32 v205, a53\n"
    "v_accvgpr_read_b32 v206, a54\n"
    "v_accvgpr_read_b32 v207, a55\n"
    "v_pk_mul_f32 v[200:201], v[200:201], v[226:227] op_sel_hi:[1,0]\n"
    "v_pk_mul_f32 v[202:203], v[202:203], v[226:227] op_sel_hi:[1,0]\n"
    "v_pk_mul_f32 v[204:205], v[204:205], v[226:227] op_sel_hi:[1,0]\n"
    "v_pk_mul_f32 v[206:207], v[206:207], v[226:227] op_sel_hi:[1,0]\n"
    "v_cvt_pk_bf16_f32 v208, v200, v201\n"
    "v_cvt_pk_bf16_f32 v209, v202, v203\n"
    "v_cvt_pk_bf16_f32 v210, v204, v205\n"
    "v_cvt_pk_bf16_f32 v211, v206, v207\n"
    "buffer_store_dwordx4 v[208:211], v2, s[36:39], s53 offen offset:2048 sc1\n"
    "v_accvgpr_read_b32 v212, a56\n"
    "v_accvgpr_read_b32 v213, a57\n"
    "v_accvgpr_read_b32 v214, a58\n"
    "v_accvgpr_read_b32 v215, a59\n"
    "v_accvgpr_read_b32 v216, a60\n"
    "v_accvgpr_read_b32 v217, a61\n"
    "v_accvgpr_read_b32 v218, a62\n"
    "v_accvgpr_read_b32 v219, a63\n"
    "v_pk_mul_f32 v[212:213], v[212:213], v[226:227] op_sel_hi:[1,0]\n"
    "v_pk_mul_f32 v[214:215], v[214:215], v[226:227] op_sel_hi:[1,0]\n"
    "v_pk_mul_f32 v[216:217], v[216:217], v[226:227] op_sel_hi:[1,0]\n"
    "v_pk_mul_f32 v[218:219], v[218:219], v[226:227] op_sel_hi:[1,0]\n"
    "v_cvt_pk_bf16_f32 v220, v212, v213\n"
    "v_cvt_pk_bf16_f32 v221, v214, v215\n"
    "v_cvt_pk_bf16_f32 v222, v216, v217\n"
    "v_cvt_pk_bf16_f32 v223, v218, v219\n"
    "buffer_store_dwordx4 v[220:223], v2, s[36:39], s53 offen offset:3072 sc1\n"
    "v_accvgpr_read_b32 v176, a64\n"
    "v_accvgpr_read_b32 v177, a65\n"
    "v_accvgpr_read_b32 v178, a66\n"
    "v_accvgpr_read_b32 v179, a67\n"
    "v_accvgpr_read_b32 v180, a68\n"
    "v_accvgpr_read_b32 v181, a69\n"
    "v_accvgpr_read_b32 v182, a70\n"
    "v_accvgpr_read_b32 v183, a71\n"
    "v_pk_mul_f32 v[176:177], v[176:177], v[228:229] op_sel_hi:[1,0]\n"
    "v_pk_mul_f32 v[178:179], v[178:179], v[228:229] op_sel_hi:[1,0]\n"
    "v_pk_mul_f32 v[180:181], v[180:181], v[228:229] op_sel_hi:[1,0]\n"
    "v_pk_mul_f32 v[182:183], v[182:183], v[228:229] op_sel_hi:[1,0]\n"
    "v_cvt_pk_bf16_f32 v184, v176, v177\n"
    "v_cvt_pk_bf16_f32 v185, v178, v179\n"
    "v_cvt_pk_bf16_f32 v186, v180, v181\n"
    "v_cvt_pk_bf16_f32 v187, v182, v183\n"
    "buffer_store_dwordx4 v[184:187], v2, s[36:39], s54 offen offset:0 sc1\n"
    "v_accvgpr_read_b32 v188, a72\n"
    "v_accvgpr_read_b32 v189, a73\n"
    "v_accvgpr_read_b32 v190, a74\n"
    "v_accvgpr_read_b32 v191, a75\n"
    "v_accvgpr_read_b32 v192, a76\n"
    "v_accvgpr_read_b32 v193, a77\n"
    "v_accvgpr_read_b32 v194, a78\n"
    "v_accvgpr_read_b32 v195, a79\n"
    "v_pk_mul_f32 v[188:189], v[188:189], v[228:229] op_sel_hi:[1,0]\n"
    "v_pk_mul_f32 v[190:191], v[190:191], v[228:229] op_sel_hi:[1,0]\n"
    "v_pk_mul_f32 v[192:193], v[192:193], v[228:229] op_sel_hi:[1,0]\n"
    "v_pk_mul_f32 v[194:195], v[194:195], v[228:229] op_sel_hi:[1,0]\n"
    "v_cvt_pk_bf16_f32 v196, v188, v189\n"
    "v_cvt_pk_bf16_f32 v197, v190, v191\n"
    "v_cvt_pk_bf16_f32 v198, v192, v193\n"
    "v_cvt_pk_bf16_f32 v199, v194, v195\n"
    "buffer_store_dwordx4 v[196:199], v2, s[36:39], s54 offen offset:1024 sc1\n"
    "v_accvgpr_read_b32 v200, a80\n"
    "v_accvgpr_read_b32 v201, a81\n"
    "v_accvgpr_read_b32 v202, a82\n"
    "v_accvgpr_read_b32 v203, a83\n"
    "v_accvgpr_read_b32 v204, a84\n"
    "v_accvgpr_read_b32 v205, a85\n"
    "v_accvgpr_read_b32 v206, a86\n"
    "v_accvgpr_read_b32 v207, a87\n"
    "v_pk_mul_f32 v[200:201], v[200:201], v[228:229] op_sel_hi:[1,0]\n"
    "v_pk_mul_f32 v[202:203], v[202:203], v[228:229] op_sel_hi:[1,0]\n"
    "v_pk_mul_f32 v[204:205], v[204:205], v[228:229] op_sel_hi:[1,0]\n"
    "v_pk_mul_f32 v[206:207], v[206:207], v[228:229] op_sel_hi:[1,0]\n"
    "v_cvt_pk_bf16_f32 v208, v200, v201\n"
    "v_cvt_pk_bf16_f32 v209, v202, v203\n"
    "v_cvt_pk_bf16_f32 v210, v204, v205\n"
    "v_cvt_pk_bf16_f32 v211, v206, v207\n"
    "buffer_store_dwordx4 v[208:211], v2, s[36:39], s54 offen offset:2048 sc1\n"
    "v_accvgpr_read_b32 v212, a88\n"
    "v_accvgpr_read_b32 v213, a89\n"
    "v_accvgpr_read_b32 v214, a90\n"
    "v_accvgpr_read_b32 v215, a91\n"
    "v_accvgpr_read_b32 v216, a92\n"
    "v_accvgpr_read_b32 v217, a93\n"
    "v_accvgpr_read_b32 v218, a94\n"
    "v_accvgpr_read_b32 v219, a95\n"
    "v_pk_mul_f32 v[212:213], v[212:213], v[228:229] op_sel_hi:[1,0]\n"
    "v_pk_mul_f32 v[214:215], v[214:215], v[228:229] op_sel_hi:[1,0]\n"
    "v_pk_mul_f32 v[216:217], v[216:217], v[228:229] op_sel_hi:[1,0]\n"
    "v_pk_mul_f32 v[218:219], v[218:219], v[228:229] op_sel_hi:[1,0]\n"
    "v_cvt_pk_bf16_f32 v220, v212, v213\n"
    "v_cvt_pk_bf16_f32 v221, v214, v215\n"
    "v_cvt_pk_bf16_f32 v222, v216, v217\n"
    "v_cvt_pk_bf16_f32 v223, v218, v219\n"
    "buffer_store_dwordx4 v[220:223], v2, s[36:39], s54 offen offset:3072 sc1\n"
    "v_accvgpr_read_b32 v176, a96\n"
    "v_accvgpr_read_b32 v177, a97\n"
    "v_accvgpr_read_b32 v178, a98\n"
    "v_accvgpr_read_b32 v179, a99\n"
    "v_accvgpr_read_b32 v180, a100\n"
    "v_accvgpr_read_b32 v181, a101\n"
    "v_accvgpr_read_b32 v182, a102\n"
    "v_accvgpr_read_b32 v183, a103\n"
    "v_pk_mul_f32 v[176:177], v[176:177], v[230:231] op_sel_hi:[1,0]\n"
    "v_pk_mul_f32 v[178:179], v[178:179], v[230:231] op_sel_hi:[1,0]\n"
    "v_pk_mul_f32 v[180:181], v[180:181], v[230:231] op_sel_hi:[1,0]\n"
    "v_pk_mul_f32 v[182:183], v[182:183], v[230:231] op_sel_hi:[1,0]\n"
    "v_cvt_pk_bf16_f32 v184, v176, v177\n"
    "v_cvt_pk_bf16_f32 v185, v178, v179\n"
    "v_cvt_pk_bf16_f32 v186, v180, v181\n"
    "v_cvt_pk_bf16_f32 v187, v182, v183\n"
    "buffer_store_dwordx4 v[184:187], v2, s[36:39], s55 offen offset:0 sc1\n"
    "v_accvgpr_read_b32 v188, a104\n"
    "v_accvgpr_read_b32 v189, a105\n"
    "v_accvgpr_read_b32 v190, a106\n"
    "v_accvgpr_read_b32 v191, a107\n"
    "v_accvgpr_read_b32 v192, a108\n"
    "v_accvgpr_read_b32 v193, a109\n"
    "v_accvgpr_read_b32 v194, a110\n"
    "v_accvgpr_read_b32 v195, a111\n"
    "v_pk_mul_f32 v[188:189], v[188:189], v[230:231] op_sel_hi:[1,0]\n"
    "v_pk_mul_f32 v[190:191], v[190:191], v[230:231] op_sel_hi:[1,0]\n"
    "v_pk_mul_f32 v[192:193], v[192:193], v[230:231] op_sel_hi:[1,0]\n"
    "v_pk_mul_f32 v[194:195], v[194:195], v[230:231] op_sel_hi:[1,0]\n"
    "v_cvt_pk_bf16_f32 v196, v188, v189\n"
    "v_cvt_pk_bf16_f32 v197, v190, v191\n"
    "v_cvt_pk_bf16_f32 v198, v192, v193\n"
    "v_cvt_pk_bf16_f32 v199, v194, v195\n"
    "buffer_store_dwordx4 v[196:199], v2, s[36:39], s55 offen offset:1024 sc1\n"
    "v_accvgpr_read_b32 v200, a112\n"
    "v_accvgpr_read_b32 v201, a113\n"
    "v_accvgpr_read_b32 v202, a114\n"
    "v_accvgpr_read_b32 v203, a115\n"
    "v_accvgpr_read_b32 v204, a116\n"
    "v_accvgpr_read_b32 v205, a117\n"
    "v_accvgpr_read_b32 v206, a118\n"
    "v_accvgpr_read_b32 v207, a119\n"
    "v_pk_mul_f32 v[200:201], v[200:201], v[230:231] op_sel_hi:[1,0]\n"
    "v_pk_mul_f32 v[202:203], v[202:203], v[230:231] op_sel_hi:[1,0]\n"
    "v_pk_mul_f32 v[204:205], v[204:205], v[230:231] op_sel_hi:[1,0]\n"
    "v_pk_mul_f32 v[206:207], v[206:207], v[230:231] op_sel_hi:[1,0]\n"
    "v_cvt_pk_bf16_f32 v208, v200, v201\n"
    "v_cvt_pk_bf16_f32 v209, v202, v203\n"
    "v_cvt_pk_bf16_f32 v210, v204, v205\n"
    "v_cvt_pk_bf16_f32 v211, v206, v207\n"
    "buffer_store_dwordx4 v[208:211], v2, s[36:39], s55 offen offset:2048 sc1\n"
    "v_accvgpr_read_b32 v212, a120\n"
    "v_accvgpr_read_b32 v213, a121\n"
    "v_accvgpr_read_b32 v214, a122\n"
    "v_accvgpr_read_b32 v215, a123\n"
    "v_accvgpr_read_b32 v216, a124\n"
    "v_accvgpr_read_b32 v217, a125\n"
    "v_accvgpr_read_b32 v218, a126\n"
    "v_accvgpr_read_b32 v219, a127\n"
    "v_pk_mul_f32 v[212:213], v[212:213], v[230:231] op_sel_hi:[1,0]\n"
    "v_pk_mul_f32 v[214:215], v[214:215], v[230:231] op_sel_hi:[1,0]\n"
    "v_pk_mul_f32 v[216:217], v[216:217], v[230:231] op_sel_hi:[1,0]\n"
    "v_pk_mul_f32 v[218:219], v[218:219], v[230:231] op_sel_hi:[1,0]\n"
    "v_cvt_pk_bf16_f32 v220, v212, v213\n"
    "v_cvt_pk_bf16_f32 v221, v214, v215\n"
    "v_cvt_pk_bf16_f32 v222, v216, v217\n"
    "v_cvt_pk_bf16_f32 v223, v218, v219\n"
    "buffer_store_dwordx4 v[220:223], v2, s[36:39], s55 offen offset:3072 sc1\n"
    "s_sub_u32 s52, s74, 27\n"
    "s_lshl_b32 s52, s52, 3\n"
    "s_add_u32 s52, s52, s75\n"
    "s_lshl_b32 s52, s52, 2\n"
    "s_add_u32 s52, s52, s73\n"
    "s_lshl_b32 s52, s52, 15\n"
    "s_add_u32 s52, s52, 33554432\n"
    "s_add_u32 s20, s60, s52\n"
    "s_addc_u32 s21, s61, 0\n"
    "s_mov_b32 s22, 32768\n"
    "s_mov_b32 s23, 0x20000\n"
    "v_mov_b32 v176, -1\n"
    "v_mov_b32 v177, -1\n"
    "v_mov_b32 v178, -1\n"
    "v_mov_b32 v179, -1\n"
    "s_mov_b32 s52, 0\n"
    "K1_MK:\n"
    "buffer_store_dwordx4 v[176:179], v2, s[20:23], s52 offen offset:0 sc1\n"
    "buffer_store_dwordx4 v[176:179], v2, s[20:23], s52 offen offset:1024 sc1\n"
    "buffer_store_dwordx4 v[176:179], v2, s[20:23], s52 offen offset:2048 sc1\n"
    "buffer_store_dwordx4 v[176:179], v2, s[20:23], s52 offen offset:3072 sc1\n"
    "s_add_u32 s52, s52, 4096\n"
    "s_cmp_lg_u32 s52, 32768\n"
    "s_cbranch_scc1 K1_MK\n"
    "K1_END:\n"
    "s_waitcnt vmcnt(0) lgkmcnt(0)\n"
    "s_mov_b32 %0, s58\n"
    : "=s"(flag) : "s"(ws),"s"(kc),"s"(vc),"s"(wq),"s"(wg),"s"(wu),"s"(blockIdx.x),"v"(threadIdx.x),"s"(wo),"s"(wd)
    : "v1","v2","v3","v4","v5","v6","v7","v8","v9","v10","v11","v12","v13","v14","v15","v16","v17","v18","v19","v20","v21","v22","v23","v24","v25","v26","v27","v28","v29","v30","v31","v32","v33","v34","v35","v36","v37","v38","v39","v40","v41","v42","v43","v44","v45","v46","v47","v48","v49","v50","v51","v52","v53","v54","v55","v56","v57","v58","v59","v60","v61","v62","v63","v64","v65","v66","v67","v68","v69","v70","v71","v72","v73","v74","v75","v76","v77","v78","v79","v80","v81","v82","v83","v84","v85","v86","v87","v88","v89","v90","v91","v92","v93","v94","v95","v96","v97","v98","v99","v100","v101","v102","v103","v104","v105","v106","v107","v108","v109","v110","v111","v112","v113","v114","v115","v116","v117","v118","v119","v120","v121","v122","v123","v124","v125","v126","v127","v128","v129","v130","v131","v132","v133","v134","v135","v136","v137","v138","v139","v140","v141","v142","v143","v144","v145","v146","v147","v148","v149","v150","v151","v152","v153","v154","v155","v156","v157","v158","v159","v160","v161","v162","v163","v164","v165","v166","v167","v168","v169","v170","v171","v172","v173","v174","v175","v176","v177","v178","v179","v180","v181","v182","v183","v184","v185","v186","v187","v188","v189","v190","v191","v192","v193","v194","v195","v196","v197","v198","v199","v200","v201","v202","v203","v204","v205","v206","v207","v208","v209","v210","v211","v212","v213","v214","v215","v216","v217","v218","v219","v220","v221","v222","v223","v224","v225","v226","v227","v228","v229","v230","v231","v232","v233","v234","v235","v236","v237","v238","v239","v240","v241","v242","v243","v244","v245","v246","v247","v248","v249","v250","v251","v252","v253","v254","v255","a0","a1","a2","a3","a4","a5","a6","a7","a8","a9","a10","a11","a12","a13","a14","a15","a16","a17","a18","a19","a20","a21","a22","a23","a24","a25","a26","a27","a28","a29","a30","a31","a32","a33","a34","a35","a36","a37","a38","a39","a40","a41","a42","a43","a44","a45","a46","a47","a48","a49","a50","a51","a52","a53","a54","a55","a56","a57","a58","a59","a60","a61","a62","a63","a64","a65","a66","a67","a68","a69","a70","a71","a72","a73","a74","a75","a76","a77","a78","a79","a80","a81","a82","a83","a84","a85","a86","a87","a88","a89","a90","a91","a92","a93","a94","a95","a96","a97","a98","a99","a100","a101","a102","a103","a104","a105","a106","a107","a108","a109","a110","a111","a112","a113","a114","a115","a116","a117","a118","a119","a120","a121","a122","a123","a124","a125","a126","a127","a128","a129","a130","a131","a132","a133","a134","a135","a136","a137","a138","a139","a140","a141","a142","a143","a144","a145","a146","a147","a148","a149","a150","a151","a152","a153","a154","a155","a156","a157","a158","a159","a160","a161","a162","a163","a164","a165","a166","a167","a168","a169","a170","a171","a172","a173","a174","a175","a176","a177","a178","a179","a180","a181","a182","a183","a184","a185","a186","a187","a188","a189","a190","a191","a192","a193","a194","a195","a196","a197","a198","a199","a200","a201","a202","a203","a204","a205","a206","a207","a208","a209","a210","a211","a212","a213","a214","a215","a216","a217","a218","a219","a220","a221","a222","a223","a224","a225","a226","a227","a228","a229","a230","a231","a232","a233","a234","a235","a236","a237","a238","a239","a240","a241","a242","a243","a244","a245","a246","a247","a248","a249","a250","a251","a252","a253","a254","a255","s20","s21","s22","s23","s24","s25","s26","s27","s28","s29","s30","s31","s33","s34","s35","s36","s37","s38","s39","s40","s41","s42","s43","s44","s45","s46","s47","s48","s49","s50","s51","s52","s53","s54","s55","s56","s57","s58","s59","s60","s61","s62","s63","s64","s65","s66","s67","s68","s69","s70","s71","s72","s73","s74","s75","s76","s77","s78","s79","s80","s81","s82","s83","s84","s85","s86","s87","s88","s89","s90","s91","s92","s93","s94","s95","s96","s97","s98","s99","vcc","scc","memory");
  if(flag) slow_attn(ws,kc,vc,wq,blockIdx.x,wv);
}

extern "C" void kernel_launch(void* const* d_in, const int* in_sizes, int n_in, void* d_out, int out_size, void* d_ws, size_t ws_size, hipStream_t stream) {
  const u16* x=(const u16*)d_in[0]; const u16* kc=(const u16*)d_in[1]; const u16* vc=(const u16*)d_in[2];
  const u16* wq=(const u16*)d_in[3]; const u16* wo=(const u16*)d_in[4];
  const u16* wg=(const u16*)d_in[6]; const u16* wu=(const u16*)d_in[7]; const u16* wd=(const u16*)d_in[8];
  char* ws=(char*)d_ws;
  hipFuncSetAttribute((const void*)k1_main,hipFuncAttributeMaxDynamicSharedMemorySize,K1_LDS);
  hipFuncSetAttribute((const void*)k2_out,hipFuncAttributeMaxDynamicSharedMemorySize,135168);
  k0_ln<<<256,256,0,stream>>>(x,(const uint4*)wq,ws);
  k1_main<<<256,256,K1_LDS,stream>>>(ws,kc,vc,wq,wg,wu,wo,wd);
  k2_out<<<240,512,135168,stream>>>(ws,wo,wd,x,(u16*)d_out);
}
```
